```python
import torch

def _softmax(x, axis=-1):
    return torch.softmax(x, dim=axis)
def _gelu(x):
    return torch.nn.functional.gelu(x, approximate="tanh")
def _silu(x):
    return torch.nn.functional.silu(x)
def _sigmoid(x):
    return torch.sigmoid(x)
def _layernorm(x, axis=-1, eps=1e-5):
    m = x.mean(dim=axis, keepdim=True)
    v = x.var(dim=axis, keepdim=True, unbiased=False)
    return (x - m) / torch.sqrt(v + eps)
def _scatter_add(tgt, idx, src, axis=0):
    return tgt.clone().index_add_(axis, idx, src)
def _segment_sum(src, seg, n):
    out = torch.zeros((n,) + src.shape[1:], dtype=src.dtype)
    return out.index_add_(0, seg, src)
def _pad(x, pw):
    flat = tuple(v for pair in reversed(pw) for v in pair)
    return torch.nn.functional.pad(x, flat)

kernel_name = 'standalone__sliding_block_cached__sampling'

def setup_inputs():

    _w = (lambda a: (lambda t: t.to(torch.bfloat16) if t.is_floating_point() else t)(torch.as_tensor(a)))
    B, Sq, D = 16, 1, 576
    Hq, Hkv, Sk, Dh = 32, 16, 4096, 128
    assert Hq % Hkv == 0, (Hq, Hkv)
    return {
        "x": _w((lambda *s: torch.randn(*s, dtype=torch.bfloat16) * 0.8)(B, Sq, D)),
        "k_cache": _w((lambda *s: torch.randn(*s, dtype=torch.bfloat16) * 0.8)(B, Hkv, Sk, Dh)),
        "v_cache": _w((lambda *s: torch.randn(*s, dtype=torch.bfloat16) * 0.05)(B, Hkv, Sk, Dh)),
        "wq": _w((lambda *s: torch.randn(*s, dtype=torch.bfloat16) * 0.8)(D, Hq * Dh) * (D ** -0.5)),
        "wo": _w((lambda *s: torch.randn(*s, dtype=torch.bfloat16) * 0.05)(Hq * Dh, D) * ((Hq * Dh) ** -0.5)),
    }

def reference(x, k_cache, v_cache, wq, wo):
    B, Sq, D = x.shape
    Hq, Dh = 32, 128
    Hkv, Sk = k_cache.shape[1], k_cache.shape[2]
    W = min(4096, max(256, Sk // 4))
    rep = Hq // Hkv
    q = (lambda x, *d: x.permute(*d))((x @ wq).reshape(B, Sq, Hq, Dh), 0, 2, 1, 3)
    k = (lambda x, n, axis: torch.repeat_interleave(x, n, dim=axis))(k_cache[:, :, Sk - W:, :], rep, axis=1)
    v = (lambda x, n, axis: torch.repeat_interleave(x, n, dim=axis))(v_cache[:, :, Sk - W:, :], rep, axis=1)
    scores = torch.einsum("bhqd,bhkd->bhqk", q, k) * (Dh ** -0.5)
    p = _softmax(scores, axis=-1)
    o = torch.einsum("bhqk,bhkd->bhqd", p, v)
    return (lambda x, *d: x.permute(*d))(o, 0, 2, 1, 3).reshape(B, Sq, Hq * Dh) @ wo
```

```cpp
#include <hip/hip_runtime.h>
#include <hip/hip_bf16.h>
#include <cstdint>

constexpr int B = 16, D = 576, HQ = 32, HKV = 16, SK = 4096, DH = 128, W = 1024;
constexpr int NQ = HQ * DH;

typedef unsigned u32x4 __attribute__((ext_vector_type(4)));
typedef unsigned u32x2 __attribute__((ext_vector_type(2)));
typedef float f32x4 __attribute__((ext_vector_type(4)));
typedef short bf16x8 __attribute__((ext_vector_type(8)));
typedef short s16x4 __attribute__((ext_vector_type(4)));
typedef __bf16 bf16x2 __attribute__((ext_vector_type(2)));
#define LAS __attribute__((address_space(3)))
#define GAS __attribute__((address_space(1)))

__device__ __forceinline__ float dot2(unsigned k, unsigned q, float acc) {
  return __builtin_amdgcn_fdot2_f32_bf16(__builtin_bit_cast(bf16x2, k), __builtin_bit_cast(bf16x2, q), acc, false);
}
__device__ __forceinline__ unsigned pk_bf16(float lo, float hi) {
  bf16x2 v; v.x = (__bf16)lo; v.y = (__bf16)hi;
  return __builtin_bit_cast(unsigned, v);
}
template <int CTRL> __device__ __forceinline__ float dpp(float x) {
  return __builtin_bit_cast(float, __builtin_amdgcn_mov_dpp(__builtin_bit_cast(int, x), CTRL, 0xf, 0xf, true));
}
template <int CTRL> __device__ __forceinline__ unsigned dppu(unsigned x) {
  return (unsigned)__builtin_amdgcn_mov_dpp((int)x, CTRL, 0xf, 0xf, true);
}
constexpr int XOR1 = 0xB1, XOR2 = 0x4E, XOR7 = 0x141, XOR8 = 0x128;
__device__ __forceinline__ float xrow_max(float x) {
  auto s = __builtin_amdgcn_permlane16_swap(__float_as_uint(x), __float_as_uint(x), false, false);
  x = fmaxf(__uint_as_float(s[0]), __uint_as_float(s[1]));
  auto t = __builtin_amdgcn_permlane32_swap(__float_as_uint(x), __float_as_uint(x), false, false);
  return fmaxf(__uint_as_float(t[0]), __uint_as_float(t[1]));
}
__device__ __forceinline__ float xrow_sum(float x) {
  auto s = __builtin_amdgcn_permlane16_swap(__float_as_uint(x), __float_as_uint(x), false, false);
  x = __uint_as_float(s[0]) + __uint_as_float(s[1]);
  auto t = __builtin_amdgcn_permlane32_swap(__float_as_uint(x), __float_as_uint(x), false, false);
  return __uint_as_float(t[0]) + __uint_as_float(t[1]);
}
__device__ __forceinline__ float wave_max(float x) {
  x = fmaxf(x, dpp<XOR1>(x)); x = fmaxf(x, dpp<XOR2>(x)); x = fmaxf(x, dpp<XOR7>(x)); x = fmaxf(x, dpp<XOR8>(x));
  return xrow_max(x);
}
__device__ __forceinline__ float wave_sum(float x) {
  x += dpp<XOR1>(x); x += dpp<XOR2>(x); x += dpp<XOR7>(x); x += dpp<XOR8>(x);
  return xrow_sum(x);
}
__device__ __forceinline__ f32x4 mfma16(u32x4 a, u32x4 b, f32x4 c) {
  return __builtin_amdgcn_mfma_f32_16x16x32_bf16(__builtin_bit_cast(bf16x8, a), __builtin_bit_cast(bf16x8, b), c, 0, 0, 0);
}
__device__ __forceinline__ u32x4 ldnt(const GAS u32x4* p) { return __builtin_nontemporal_load(p); }

__global__ void __launch_bounds__(64) qproj_kernel(const unsigned short* __restrict__ x, const unsigned short* __restrict__ wq,
                                                   unsigned short* __restrict__ qws) {
  __shared__ __attribute__((aligned(16))) unsigned char lds[576 * 32];
  const int l = threadIdx.x, g = l >> 4, c = l & 15, col0 = blockIdx.x * 16;
  u32x4 wr[18], xr[18];
#pragma unroll
  for (int s = 0; s < 18; ++s) wr[s] = *(const u32x4*)(wq + (size_t)(32 * s + (l >> 1)) * NQ + col0 + 8 * (l & 1));
#pragma unroll
  for (int s = 0; s < 18; ++s) xr[s] = *(const u32x4*)(x + c * D + 32 * s + 8 * g);
#pragma unroll
  for (int s = 0; s < 18; ++s) *(u32x4*)(lds + s * 1024 + l * 16) = wr[s];
  __syncthreads();
  f32x4 acc = {0.f, 0.f, 0.f, 0.f};
  const int troff = (8 * g + (c >> 2)) * 32 + (c & 3) * 8;
#pragma unroll
  for (int s = 0; s < 18; ++s) {
    s16x4 lo = __builtin_amdgcn_ds_read_tr16_b64_v4i16((LAS s16x4*)(lds + s * 1024 + troff));
    s16x4 hi = __builtin_amdgcn_ds_read_tr16_b64_v4i16((LAS s16x4*)(lds + s * 1024 + troff + 128));
    u32x2 a = __builtin_bit_cast(u32x2, lo), b2 = __builtin_bit_cast(u32x2, hi);
    u32x4 bf = {a.x, a.y, b2.x, b2.y};
    acc = mfma16(xr[s], bf, acc);
  }
#pragma unroll
  for (int r = 0; r < 4; ++r) qws[(4 * g + r) * NQ + col0 + c] = (unsigned short)(pk_bf16(acc[r], 0.f) & 0xffffu);
}

__global__ void __launch_bounds__(512, 2) attn_kernel(const unsigned short* __restrict__ kc, const unsigned short* __restrict__ vc,
                                                      const unsigned short* __restrict__ qws, unsigned short* __restrict__ ows) {
  __shared__ __attribute__((aligned(16))) float red[8 * 256 + 64];
  const int bid = blockIdx.x;
  const int kvh = (bid & 7) * 2 + ((bid >> 3) & 1), b = bid >> 4;
  const int tid = threadIdx.x, l = tid & 63, w = __builtin_amdgcn_readfirstlane(tid >> 6), g = l >> 4, c = l & 15;
  const size_t base = ((size_t)(b * HKV + kvh) * SK + (SK - W) + w * 128) * DH;
  const GAS u32x4* kp = (const GAS u32x4*)(kc + base) + l;
  const GAS u32x4* vp = (const GAS u32x4*)(vc + base) + l;
  u32x4 q0 = *(const u32x4*)(qws + b * NQ + (2 * kvh) * DH + 8 * c);
  u32x4 q1 = *(const u32x4*)(qws + b * NQ + (2 * kvh + 1) * DH + 8 * c);
  u32x4 Kr[32], Vr[32];
#pragma unroll
  for (int i = 0; i < 32; ++i) Kr[i] = ldnt(kp + i * 64);
#pragma unroll
  for (int i = 0; i < 16; ++i) Vr[i] = ldnt(vp + i * 64);
  __builtin_amdgcn_sched_barrier(0);
  float sc[2][2];
#pragma unroll
  for (int hf = 0; hf < 2; ++hf) {
    float x0[16], x1[16];
#pragma unroll
    for (int i = 0; i < 16; ++i) {
      const u32x4 kk = Kr[hf * 16 + i];
      float a = dot2(kk.x, q0.x, 0.f), bb = dot2(kk.x, q1.x, 0.f);
      a = dot2(kk.y, q0.y, a); bb = dot2(kk.y, q1.y, bb);
      a = dot2(kk.z, q0.z, a); bb = dot2(kk.z, q1.z, bb);
      a = dot2(kk.w, q0.w, a); bb = dot2(kk.w, q1.w, bb);
      x0[i] = a; x1[i] = bb;
    }
    if (hf == 0) {
      __builtin_amdgcn_sched_barrier(0);
#pragma unroll
      for (int i = 16; i < 32; ++i) Vr[i] = ldnt(vp + i * 64);
      __builtin_amdgcn_sched_barrier(0);
    }
#define TR_STEP(HALF, CTRL, BIT)                                                              \
  _Pragma("unroll") for (int h = 0; h < (HALF); ++h) {                                        \
    const bool up = (c & (BIT)) != 0;                                                         \
    const float k0 = up ? x0[h + (HALF)] : x0[h], s0 = up ? x0[h] : x0[h + (HALF)];           \
    const float k1 = up ? x1[h + (HALF)] : x1[h], s1 = up ? x1[h] : x1[h + (HALF)];           \
    x0[h] = k0 + dpp<CTRL>(s0); x1[h] = k1 + dpp<CTRL>(s1);                                   \
  }
    TR_STEP(8, XOR8, 8) TR_STEP(4, XOR7, 4) TR_STEP(2, XOR2, 2) TR_STEP(1, XOR1, 1)
#undef TR_STEP
    sc[hf][0] = x0[0]; sc[hf][1] = x1[0];
  }
  constexpr float C2 = 0.088388347648318440f * 1.4426950408889634f;
  float m[2], lsum[2], p[2][2];
#pragma unroll
  for (int h = 0; h < 2; ++h) {
    const float t0 = sc[0][h] * C2, t1 = sc[1][h] * C2;
    m[h] = wave_max(fmaxf(t0, t1));
    p[0][h] = __builtin_amdgcn_exp2f(t0 - m[h]); p[1][h] = __builtin_amdgcn_exp2f(t1 - m[h]);
    lsum[h] = wave_sum(p[0][h] + p[1][h]);
  }
  const unsigned P0 = pk_bf16(p[0][0], p[0][1]), P1 = pk_bf16(p[1][0], p[1][1]);
  const int hh = c >> 3, jj = c & 7;
  const unsigned pick = (unsigned)((2 * hh + 1) << 8 | (2 * hh));
  const unsigned act = (jj & 1) ? (pick << 16 | 0x0C0Cu) : (0x0C0C0000u | pick);
  u32x4 sel;
  sel.x = (jj >> 1) == 0 ? act : 0x0C0C0C0Cu; sel.y = (jj >> 1) == 1 ? act : 0x0C0C0C0Cu;
  sel.z = (jj >> 1) == 2 ? act : 0x0C0C0C0Cu; sel.w = (jj >> 1) == 3 ? act : 0x0C0C0C0Cu;
  f32x4 acc = {0.f, 0.f, 0.f, 0.f};
#define PV_STEP(I)                                                                            \
  {                                                                                           \
    const unsigned t = dppu<0x150 + ((I) & 15)>((I) < 16 ? P0 : P1);                          \
    u32x4 a;                                                                                  \
    a.x = __builtin_amdgcn_perm(t, t, sel.x); a.y = __builtin_amdgcn_perm(t, t, sel.y);       \
    a.z = __builtin_amdgcn_perm(t, t, sel.z); a.w = __builtin_amdgcn_perm(t, t, sel.w);       \
    acc = mfma16(a, Vr[I], acc);                                                              \
  }
#define PV4(I) PV_STEP(I) PV_STEP(I + 1) PV_STEP(I + 2) PV_STEP(I + 3)
  PV4(0) PV4(4) PV4(8) PV4(12) PV4(16) PV4(20) PV4(24) PV4(28)
#undef PV4
#undef PV_STEP
  *(f32x4*)(red + w * 256 + (g >> 1) * 128 + 8 * c + 4 * (g & 1)) = acc;
  if (l == 0) { f32x4 ml = {m[0], m[1], lsum[0], lsum[1]}; *(f32x4*)(red + 2048 + w * 4) = ml; }
  __syncthreads();
  if (tid < 256) {
    const int h = tid >> 7;
    float M = -INFINITY;
#pragma unroll
    for (int ww = 0; ww < 8; ++ww) M = fmaxf(M, red[2048 + ww * 4 + h]);
    float L = 0.f, O = 0.f;
#pragma unroll
    for (int ww = 0; ww < 8; ++ww) {
      const float e = __builtin_amdgcn_exp2f(red[2048 + ww * 4 + h] - M);
      L = fmaf(e, red[2048 + ww * 4 + 2 + h], L);
      O = fmaf(e, red[ww * 256 + tid], O);
    }
    ows[b * NQ + kvh * 256 + tid] = (unsigned short)(pk_bf16(O / L, 0.f) & 0xffffu);
  }
}

__global__ void __launch_bounds__(256) oproj_kernel(const unsigned short* __restrict__ ows, const unsigned short* __restrict__ wo,
                                                    unsigned short* __restrict__ out) {
  __shared__ float red[256];
  const int b = blockIdx.x / 9, cg = blockIdx.x % 9, tid = threadIdx.x, col = cg * 64 + (tid & 63), kq = tid >> 6;
  float acc = 0.f;
  for (int k = kq * 1024; k < kq * 1024 + 1024; ++k) {
    const float o = __uint_as_float((unsigned)ows[b * NQ + k] << 16);
    const float wv = __uint_as_float((unsigned)wo[(size_t)k * D + col] << 16);
    acc = fmaf(o, wv, acc);
  }
  red[tid] = acc;
  __syncthreads();
  if (tid < 64) {
    const float s = (red[tid] + red[tid + 64]) + (red[tid + 128] + red[tid + 192]);
    out[b * D + col] = (unsigned short)(pk_bf16(s, 0.f) & 0xffffu);
  }
}

extern "C" void kernel_launch(void* const* d_in, const int* in_sizes, int n_in, void* d_out, int out_size, void* d_ws,
                              size_t ws_size, hipStream_t stream) {
  const unsigned short* x = (const unsigned short*)d_in[0];
  const unsigned short* kc = (const unsigned short*)d_in[1];
  const unsigned short* vc = (const unsigned short*)d_in[2];
  const unsigned short* wq = (const unsigned short*)d_in[3];
  const unsigned short* wo = (const unsigned short*)d_in[4];
  unsigned short* qws = (unsigned short*)d_ws;
  unsigned short* ows = qws + B * NQ;
  qproj_kernel<<<256, 64, 0, stream>>>(x, wq, qws);
  attn_kernel<<<256, 512, 0, stream>>>(kc, vc, qws, ows);
  oproj_kernel<<<B * 9, 256, 0, stream>>>(ows, wo, (unsigned short*)d_out);
}
```

```cpp
#include <hip/hip_runtime.h>
#include <hip/hip_bf16.h>
#include <cstdint>

#define DIAG 0
#define P_WIN 4
#define P_KB 24
#define P_CHUNK 8

#define AUX_WQ 2
#define AUX_X 2
#define AUX_WO 2
#define NO_WQ 0
#define NO_X 0
#define NO_WO 0
constexpr int B = 16, D = 576, HQ = 32, HKV = 16, SK = 4096, DH = 128, W = 1024;
constexpr int NQ = HQ * DH;
constexpr int WIN = P_WIN;
constexpr int KB = P_KB;
constexpr int CHUNK = P_CHUNK;
constexpr int NWAVES = 9;

typedef unsigned u32x4 __attribute__((ext_vector_type(4)));
typedef unsigned u32x2 __attribute__((ext_vector_type(2)));
typedef float f32x4 __attribute__((ext_vector_type(4)));
typedef float f32x2 __attribute__((ext_vector_type(2)));
typedef short bf16x8 __attribute__((ext_vector_type(8)));
typedef short s16x4 __attribute__((ext_vector_type(4)));
typedef __bf16 bf16x2 __attribute__((ext_vector_type(2)));
#define LAS __attribute__((address_space(3)))
#define GAS __attribute__((address_space(1)))

constexpr int AUX_SC1 = 16, AUX_NT = 2;
constexpr int RSRC_W3 = 0x00020000;

constexpr int WS_EPOCH = 0;
constexpr int WS_QBOX = 4096;
constexpr int WS_OBOX = WS_QBOX + 16 * 16 * 1024;
constexpr int WS_PBOX = WS_OBOX + 16 * 16 * 1024;
constexpr int WS_END = WS_PBOX + 16 * 16 * 288 * 16;
constexpr int WS_DIAG = 8 << 20;
constexpr unsigned SPIN_CAP = 1u << 18;
constexpr int bitrev4(int i) { return ((i & 1) << 3) | ((i & 2) << 1) | ((i & 4) >> 1) | ((i & 8) >> 3); }

constexpr int L_RED = 0;
constexpr int L_ML = 8192;
constexpr int L_Q = L_ML + 128;
constexpr int L_TAG = L_Q + 512;
constexpr int L_FLAG = L_TAG + 16;
constexpr int L_RED2 = 9216;
constexpr int L_WQ = L_RED2;
constexpr int L_WO = L_RED2 + 24576;
constexpr int L_BYTES = L_WO + 24576;

__device__ __forceinline__ float dot2(unsigned k, unsigned q, float acc) {
  return __builtin_amdgcn_fdot2_f32_bf16(__builtin_bit_cast(bf16x2, k), __builtin_bit_cast(bf16x2, q), acc, false);
}
__device__ __forceinline__ unsigned pk_bf16(float lo, float hi) {
  bf16x2 v; v.x = (__bf16)lo; v.y = (__bf16)hi;
  return __builtin_bit_cast(unsigned, v);
}
template <int CTRL> __device__ __forceinline__ float dpp(float x) {
  return __builtin_bit_cast(float, __builtin_amdgcn_mov_dpp(__builtin_bit_cast(int, x), CTRL, 0xf, 0xf, true));
}
template <int CTRL> __device__ __forceinline__ unsigned dppu(unsigned x) {
  return (unsigned)__builtin_amdgcn_mov_dpp((int)x, CTRL, 0xf, 0xf, true);
}
constexpr int XOR1 = 0xB1, XOR2 = 0x4E, XOR7 = 0x141, XOR8 = 0x128, ROR4 = 0x124;
__device__ __forceinline__ float xrow_max(float x) {
  auto s = __builtin_amdgcn_permlane16_swap(__float_as_uint(x), __float_as_uint(x), false, false);
  x = fmaxf(__uint_as_float(s[0]), __uint_as_float(s[1]));
  auto t = __builtin_amdgcn_permlane32_swap(__float_as_uint(x), __float_as_uint(x), false, false);
  return fmaxf(__uint_as_float(t[0]), __uint_as_float(t[1]));
}
__device__ __forceinline__ float xrow_sum(float x) {
  auto s = __builtin_amdgcn_permlane16_swap(__float_as_uint(x), __float_as_uint(x), false, false);
  x = __uint_as_float(s[0]) + __uint_as_float(s[1]);
  auto t = __builtin_amdgcn_permlane32_swap(__float_as_uint(x), __float_as_uint(x), false, false);
  return __uint_as_float(t[0]) + __uint_as_float(t[1]);
}
__device__ __forceinline__ float wave_max(float x) {
  x = fmaxf(x, dpp<XOR1>(x)); x = fmaxf(x, dpp<XOR2>(x)); x = fmaxf(x, dpp<XOR7>(x)); x = fmaxf(x, dpp<XOR8>(x));
  return xrow_max(x);
}
__device__ __forceinline__ float wave_sum(float x) {
  x += dpp<XOR1>(x); x += dpp<XOR2>(x); x += dpp<XOR7>(x); x += dpp<XOR8>(x);
  return xrow_sum(x);
}
__device__ __forceinline__ f32x4 mfma16(u32x4 a, u32x4 b, f32x4 c) {
  return __builtin_amdgcn_mfma_f32_16x16x32_bf16(__builtin_bit_cast(bf16x8, a), __builtin_bit_cast(bf16x8, b), c, 0, 0, 0);
}
__device__ __forceinline__ __amdgpu_buffer_rsrc_t make_rsrc(const void* p, int bytes) {
  return __builtin_amdgcn_make_buffer_rsrc((void*)p, 0, bytes, RSRC_W3);
}
__device__ __forceinline__ u32x4 tr_frag(LAS unsigned char* p, int second) {
  s16x4 lo = __builtin_amdgcn_ds_read_tr16_b64_v4i16((LAS s16x4*)p);
  s16x4 hi = __builtin_amdgcn_ds_read_tr16_b64_v4i16((LAS s16x4*)(p + second));
  u32x2 a = __builtin_bit_cast(u32x2, lo), b = __builtin_bit_cast(u32x2, hi);
  return u32x4{a.x, a.y, b.x, b.y};
}
__device__ __forceinline__ unsigned bad_unit(u32x4 v, unsigned ta, unsigned tb) { return (v.y ^ ta) | (v.w ^ tb); }
__device__ __forceinline__ void st_unit(__amdgpu_buffer_rsrc_t r, int off, u32x4 v) { __builtin_amdgcn_raw_buffer_store_b128(v, r, off, 0, AUX_SC1); }
#define SB() __builtin_amdgcn_sched_barrier(0)
#define CFENCE() asm volatile("" ::: "memory")
#define BARRIER() do { CFENCE(); asm volatile("s_waitcnt lgkmcnt(0)" ::: "memory"); __builtin_amdgcn_s_barrier(); CFENCE(); SB(); } while (0)
template <int N> __device__ __forceinline__ void wait_vm() { asm volatile("s_waitcnt vmcnt(%0)" ::"n"(N) : "memory"); }

#define STAMP(i) do { } while (0)

struct QKState { float u0[4], u1[4], sc[2][2]; };

__global__ void __launch_bounds__(NWAVES * 64)
fused_kernel(const unsigned short* __restrict__ x, const unsigned short* __restrict__ kc, const unsigned short* __restrict__ vc,
             const unsigned short* __restrict__ wq, const unsigned short* __restrict__ wo, unsigned short* __restrict__ out,
             unsigned char* __restrict__ ws) {
  __shared__ __attribute__((aligned(16))) unsigned char smem[L_BYTES];
  LAS unsigned char* L = (LAS unsigned char*)smem;
  const int bid = blockIdx.x;
  const int kvh = (bid & 7) * 2 + ((bid >> 3) & 1), b = bid >> 4;
  const int tid = threadIdx.x, l = tid & 63, w = __builtin_amdgcn_readfirstlane(tid >> 6), g = l >> 4, c = l & 15;
  const int sw = w - 1;
  STAMP(0);
  const __amdgpu_buffer_rsrc_t rws = make_rsrc(ws, WS_END);
  const int l16 = l * 16;
  const bool oproj = b < 12;
  unsigned ta, tb;

  if (w == 0) {
    __builtin_amdgcn_s_setprio(3);
    const u32x2 ep = __builtin_amdgcn_raw_buffer_load_b64(rws, 0, WS_EPOCH, AUX_SC1);
    if (l == 0) *(LAS unsigned*)(L + L_FLAG) = 0u;
    BARRIER();
    const int col0 = kvh * 256 + b * 16;
    const __amdgpu_buffer_rsrc_t rwq = make_rsrc(wq + col0, NO_WQ ? 0 : D * NQ * 2 - col0 * 2);
    const __amdgpu_buffer_rsrc_t rx = make_rsrc(x, NO_X ? 0 : B * D * 2);
    u32x4 wr[18], xr[18];
    const int wqoff = (l >> 1) * (NQ * 2) + (l & 1) * 16;
#pragma unroll
    for (int s = 0; s < 18; ++s) { wr[s] = __builtin_amdgcn_raw_buffer_load_b128(rwq, wqoff, s * 32 * NQ * 2, AUX_WQ); SB(); }
    const int xoff = c * (D * 2) + g * 16;
#pragma unroll
    for (int s = 0; s < 18; ++s) { xr[s] = __builtin_amdgcn_raw_buffer_load_b128(rx, xoff + s * 64, 0, AUX_X); SB(); }
    STAMP(1);
#pragma unroll
    for (int s = 0; s < 18; ++s) *(LAS u32x4*)(L + L_WQ + s * 1024 + l16) = wr[s];
    f32x4 acc = {0.f, 0.f, 0.f, 0.f};
    const int troff = (8 * g + (c >> 2)) * 32 + (c & 3) * 8;
#pragma unroll
    for (int s = 0; s < 18; ++s) acc = mfma16(tr_frag(L + L_WQ + s * 1024 + troff, 128), xr[s], acc);
    const unsigned long long e1 = (((unsigned long long)(unsigned)__builtin_amdgcn_readfirstlane(ep.y) << 32) |
                                   (unsigned)__builtin_amdgcn_readfirstlane(ep.x)) + 1ull;
    ta = (unsigned)e1; tb = (unsigned)(e1 >> 32) ^ (ta * 0x9E3779B1u);
    STAMP(2);
    st_unit(rws, WS_QBOX + kvh * 16384 + c * 1024 + (b * 4 + g) * 16, u32x4{pk_bf16(acc[0], acc[1]), ta, pk_bf16(acc[2], acc[3]), tb});
    STAMP(3);
    u32x4 a0;
    for (unsigned spins = 0;; ++spins) {
      CFENCE();
      a0 = __builtin_amdgcn_raw_buffer_load_b128(rws, l16, WS_QBOX + (kvh * 16 + b) * 1024, AUX_SC1);
      if (__all(bad_unit(a0, ta, tb) == 0u) || spins > SPIN_CAP) break;
    }
    *(LAS u32x2*)(L + L_Q + l * 8) = u32x2{a0.x, a0.z};
    if (l == 0) *(LAS u32x4*)(L + L_TAG) = u32x4{ta, tb, (unsigned)e1, (unsigned)(e1 >> 32)};
    asm volatile("s_waitcnt lgkmcnt(0)" ::: "memory");
    if (l == 0) *(volatile LAS unsigned*)(L + L_FLAG) = 1u;
    STAMP(4);
    __builtin_amdgcn_s_setprio(0);
    if (oproj) {
      const __amdgpu_buffer_rsrc_t rwo = make_rsrc(wo + (size_t)(kvh * 256) * D + b * 48, NO_WO ? 0 : 256 * D * 2 - b * 96);
      u32x4 wor[24];
#pragma unroll
      for (int j = 0; j < 3; ++j) {
        const int pc = j * 64 + l, row = pc / 6, ch = pc - row * 6;
#pragma unroll
        for (int s = 0; s < 8; ++s) { wor[s * 3 + j] = __builtin_amdgcn_raw_buffer_load_b128(rwo, row * (D * 2) + ch * 16, s * 32 * D * 2, AUX_WO); SB(); }
      }
#pragma unroll
      for (int j = 0; j < 24; ++j) *(LAS u32x4*)(L + L_WO + j * 1024 + l16) = wor[j];
    }
    STAMP(5);
    BARRIER();
    STAMP(6);
    const int h = l >> 5;
    float mw[8], lw[8];
    f32x4 ow[8];
#pragma unroll
    for (int ww = 0; ww < 8; ++ww) {
      mw[ww] = *(LAS float*)(L + L_ML + ww * 16 + h * 4); lw[ww] = *(LAS float*)(L + L_ML + ww * 16 + 8 + h * 4);
      ow[ww] = *(LAS f32x4*)(L + L_RED + ww * 1024 + l16);
    }
    float M = mw[0];
#pragma unroll
    for (int ww = 1; ww < 8; ++ww) M = fmaxf(M, mw[ww]);
    float Ls = 0.f;
    f32x4 O = {0.f, 0.f, 0.f, 0.f};
#pragma unroll
    for (int ww = 0; ww < 8; ++ww) {
      const float e = __builtin_amdgcn_exp2f(mw[ww] - M);
      Ls = fmaf(e, lw[ww], Ls);
      O += e * ow[ww];
    }
    const float inv = 1.0f / Ls;
    st_unit(rws, WS_OBOX + (kvh * 16 + b) * 1024 + (l >> 3) * 128 + (l & 1) * 64 + ((l >> 1) & 3) * 16,
            u32x4{pk_bf16(O.x * inv, O.y * inv), ta, pk_bf16(O.z * inv, O.w * inv), tb});
    STAMP(7);
    if (oproj) BARRIER();
  } else {
    const size_t kvbase = ((size_t)(b * HKV + kvh) * SK + (SK - W) + sw * 4) * DH;
    const __amdgpu_buffer_rsrc_t rk = make_rsrc(kc + kvbase, (W - sw * 4) * DH * 2);
    const __amdgpu_buffer_rsrc_t rv = make_rsrc(vc + kvbase, (W - sw * 4) * DH * 2);
    u32x4 R[64];
#define ISSUE(j) do { R[j] = __builtin_amdgcn_raw_buffer_load_b128((j) < 32 ? rk : rv, l16, ((j) & 31) * 8192, AUX_NT); SB(); } while (0)
#pragma unroll
    for (int j = 0; j < KB; ++j) {
      if (j >= WIN) { wait_vm<WIN - 1>(); SB(); }
      ISSUE(j);
      if (j == WIN - 1) { CFENCE(); __builtin_amdgcn_s_barrier(); CFENCE(); SB(); }
    }
    STAMP(1);
    for (unsigned spins = 0; *(volatile LAS unsigned*)(L + L_FLAG) == 0u && spins < (SPIN_CAP << 4); ++spins) __builtin_amdgcn_s_sleep(1);
    CFENCE();
    STAMP(4);
    const u32x4 q0 = *(LAS u32x4*)(L + L_Q + c * 16), q1 = *(LAS u32x4*)(L + L_Q + 256 + c * 16);
    const u32x4 etag = *(LAS u32x4*)(L + L_TAG);
    const bool up8 = (c & 8) != 0, up4 = (c & 4) != 0, up2 = (c & 2) != 0, up1 = (c & 1) != 0;
    float u0[4], u1[4], sc[2][2];
    const int hh = c >> 3, jj = c & 7;
    const unsigned pick = (unsigned)((2 * hh + 1) << 8 | (2 * hh));
    const unsigned act = (jj & 1) ? (pick << 16 | 0x0C0Cu) : (0x0C0C0000u | pick);
    u32x4 sel;
    sel.x = (jj >> 1) == 0 ? act : 0x0C0C0C0Cu; sel.y = (jj >> 1) == 1 ? act : 0x0C0C0C0Cu;
    sel.z = (jj >> 1) == 2 ? act : 0x0C0C0C0Cu; sel.w = (jj >> 1) == 3 ? act : 0x0C0C0C0Cu;
    unsigned P0 = 0, P1 = 0;
    f32x4 acc = {0.f, 0.f, 0.f, 0.f};
    u32x4 afr = {0u, 0u, 0u, 0u};
    constexpr float C2 = 0.088388347648318440f * 1.4426950408889634f;
#define COMB(UP, CTRL, LO, HI) ((UP ? HI : LO) + dpp<CTRL>(UP ? LO : HI))
#define AFRAG(I) do { const unsigned t_ = dppu<0x150 + bitrev4((I) & 15)>((I) < 16 ? P0 : P1);                                   \
      afr.x = __builtin_amdgcn_perm(t_, t_, sel.x); afr.y = __builtin_amdgcn_perm(t_, t_, sel.y);                                \
      afr.z = __builtin_amdgcn_perm(t_, t_, sel.z); afr.w = __builtin_amdgcn_perm(t_, t_, sel.w); } while (0)
    auto process = [&](auto ic) {
      constexpr int i = decltype(ic)::value;
      if constexpr (i < 32) {
        const u32x4 kk = R[i];
        float a = dot2(kk.x, q0.x, 0.f), bb = dot2(kk.x, q1.x, 0.f);
        a = dot2(kk.y, q0.y, a); bb = dot2(kk.y, q1.y, bb);
        a = dot2(kk.z, q0.z, a); bb = dot2(kk.z, q1.z, bb);
        a = dot2(kk.w, q0.w, a); bb = dot2(kk.w, q1.w, bb);
        if constexpr (!(i & 1)) { u0[0] = a; u1[0] = bb; }
        else {
          a = COMB(up8, XOR8, u0[0], a); bb = COMB(up8, XOR8, u1[0], bb);
          if constexpr (!(i & 2)) { u0[1] = a; u1[1] = bb; }
          else {
            a = COMB(up4, XOR7, u0[1], a); bb = COMB(up4, XOR7, u1[1], bb);
            if constexpr (!(i & 4)) { u0[2] = a; u1[2] = bb; }
            else {
              a = COMB(up2, XOR2, u0[2], a); bb = COMB(up2, XOR2, u1[2], bb);
              if constexpr (!(i & 8)) { u0[3] = a; u1[3] = bb; }
              else {
                a = COMB(up1, XOR1, u0[3], a); bb = COMB(up1, XOR1, u1[3], bb);
                sc[i >> 4][0] = a; sc[i >> 4][1] = bb;
              }
            }
          }
        }
        if constexpr (i == 31) {
          float m[2], lsum[2], p[2][2];
#pragma unroll
          for (int h = 0; h < 2; ++h) {
            const float t0 = sc[0][h] * C2, t1 = sc[1][h] * C2;
            m[h] = wave_max(fmaxf(t0, t1));
            p[0][h] = __builtin_amdgcn_exp2f(t0 - m[h]); p[1][h] = __builtin_amdgcn_exp2f(t1 - m[h]);
            lsum[h] = wave_sum(p[0][h] + p[1][h]);
          }
          P0 = pk_bf16(p[0][0], p[0][1]); P1 = pk_bf16(p[1][0], p[1][1]);
          if (l == 0) *(LAS f32x4*)(L + L_ML + sw * 16) = f32x4{m[0], m[1], lsum[0], lsum[1]};
          AFRAG(0);
          STAMP(5);
        }
      } else {
        acc = mfma16(afr, R[i], acc);
        if constexpr (i < 63) AFRAG(i - 31);
      }
      SB();
    };
    auto phase_b = [&](auto self, auto jc, auto dc) {
      constexpr int j = decltype(jc)::value, done = decltype(dc)::value;
      if constexpr (done < 64) {
        constexpr int avail = j < 64 ? j - WIN : 64;
        constexpr int upto = (done + CHUNK < avail) ? done + CHUNK : avail;
        auto run = [&](auto self2, auto ic) {
          constexpr int i = decltype(ic)::value;
          if constexpr (i < upto) { process(ic); self2(self2, std::integral_constant<int, i + 1>{}); }
        };
        run(run, std::integral_constant<int, done>{});
        if constexpr (j < 64) { wait_vm<WIN - 1>(); SB(); ISSUE(j); }
        self(self, std::integral_constant<int, (j < 64 ? j + 1 : 64)>{}, std::integral_constant<int, (upto > done ? upto : done)>{});
      }
    };
    phase_b(phase_b, std::integral_constant<int, KB>{}, std::integral_constant<int, 0>{});
#undef COMB
#undef AFRAG
#undef ISSUE
    *(LAS f32x4*)(L + L_RED + sw * 1024 + (g >> 1) * 512 + c * 32 + (g & 1) * 16) = acc;
    STAMP(6);
    BARRIER();
    ta = etag.x; tb = etag.y;
    if (oproj) {
      u32x4 wfr[3];
#pragma unroll
      for (int t = 0; t < 3; ++t) wfr[t] = tr_frag(L + L_WO + sw * 3072 + (8 * g + (c >> 2)) * 96 + t * 32 + (c & 3) * 8, 4 * 96);
      u32x4 a0, a1;
      {
        bool ok = false;
        const int off = c * 1024 + sw * 128 + g * 16;
        for (unsigned spins = 0;; ++spins) {
          CFENCE();
          if (!ok) {
            a0 = __builtin_amdgcn_raw_buffer_load_b128(rws, off, WS_OBOX + kvh * 16384, AUX_SC1);
            a1 = __builtin_amdgcn_raw_buffer_load_b128(rws, off + 64, WS_OBOX + kvh * 16384, AUX_SC1);
            ok = (bad_unit(a0, ta, tb) | bad_unit(a1, ta, tb)) == 0u;
          }
          if (__all(ok) || spins > SPIN_CAP) break;
          __builtin_amdgcn_s_sleep(2);
        }
      }
      STAMP(8);
      const u32x4 of = {a0.x, a0.z, a1.x, a1.z};
#pragma unroll
      for (int t = 0; t < 3; ++t) {
        const f32x4 d = mfma16(wfr[t], of, f32x4{0.f, 0.f, 0.f, 0.f});
        *(LAS f32x4*)(L + L_RED2 + (sw * 3 + t) * 1024 + l16) = d;
      }
      BARRIER();
      if (sw < 6) {
        const int t = sw >> 1, hf = sw & 1;
        f32x2 s = *(LAS f32x2*)(L + L_RED2 + t * 1024 + l16 + hf * 8);
#pragma unroll
        for (int ww = 1; ww < 8; ++ww) s += *(LAS f32x2*)(L + L_RED2 + (ww * 3 + t) * 1024 + l16 + hf * 8);
        st_unit(rws, WS_PBOX + ((c * 16 + kvh) * 288 + b * 24 + t * 8 + g * 2 + hf) * 16, u32x4{__float_as_uint(s.x), ta, __float_as_uint(s.y), tb});
        STAMP(9);
        u32x4 pv;
        bool ok = false;
        const int off = ((kvh * 16 + (l >> 2)) * 288 + b * 24 + sw * 4 + (l & 3)) * 16;
        for (unsigned spins = 0;; ++spins) {
          CFENCE();
          if (!ok) {
            pv = __builtin_amdgcn_raw_buffer_load_b128(rws, off, WS_PBOX, AUX_SC1);
            ok = bad_unit(pv, ta, tb) == 0u;
          }
          if (__all(ok) || spins > SPIN_CAP) break;
          __builtin_amdgcn_s_sleep(2);
        }
        float v0 = __uint_as_float(pv.x), v1 = __uint_as_float(pv.z);
        v0 += dpp<XOR8>(v0); v1 += dpp<XOR8>(v1);
        v0 += dpp<ROR4>(v0); v1 += dpp<ROR4>(v1);
        v0 = xrow_sum(v0); v1 = xrow_sum(v1);
        if (l < 4) *(unsigned*)(out + kvh * D + b * 48 + sw * 8 + l * 2) = pk_bf16(v0, v1);
        if (bid == 0 && tid == 64) __builtin_amdgcn_raw_buffer_store_b64(u32x2{etag.z, etag.w}, rws, 0, WS_EPOCH, AUX_SC1);
        STAMP(10);
      }
    }
  }
}

extern "C" void kernel_launch(void* const* d_in, const int* in_sizes, int n_in, void* d_out, int out_size, void* d_ws,
                              size_t ws_size, hipStream_t stream) {
  fused_kernel<<<256, NWAVES * 64, 0, stream>>>((const unsigned short*)d_in[0], (const unsigned short*)d_in[1], (const unsigned short*)d_in[2],
                                                (const unsigned short*)d_in[3], (const unsigned short*)d_in[4], (unsigned short*)d_out,
                                                (unsigned char*)d_ws);
}
```

```cpp
#include <hip/hip_runtime.h>
#include <hip/hip_bf16.h>
#include <cstdint>
#include <type_traits>

constexpr int P_PACE = 120;
constexpr int P_PACE0 = 150;
constexpr int P_PACE1 = 125;
constexpr int P_TPOLL = 660;
constexpr int P_D1 = 14;
constexpr int P_D2 = 25;

constexpr int B = 16, D = 576, HQ = 32, HKV = 16, SK = 4096, DH = 128, W = 1024;
constexpr int NQ = HQ * DH;
constexpr int NWAVES = 9;
constexpr int W0 = 4;
constexpr int WIN = 6;
constexpr int KB = 30;
constexpr int CHUNK = 3;

typedef unsigned u32x4 __attribute__((ext_vector_type(4)));
typedef unsigned u32x2 __attribute__((ext_vector_type(2)));
typedef float f32x4 __attribute__((ext_vector_type(4)));
typedef short bf16x8 __attribute__((ext_vector_type(8)));
typedef short s16x4 __attribute__((ext_vector_type(4)));
typedef __bf16 bf16x2 __attribute__((ext_vector_type(2)));
#define LAS __attribute__((address_space(3)))

constexpr int AUX_SC1 = 16, AUX_NT = 2;
constexpr int RSRC_W3 = 0x00020000;

constexpr int WS_EPOCH = 0;
constexpr int WS_TBOX = 1024;
constexpr int WS_QBOX = 4096;
constexpr int WS_OBOX = WS_QBOX + 16 * 16 * 1024;
constexpr int WS_PBOX = WS_OBOX + 16 * 16 * 1024;
constexpr int WS_END = WS_PBOX + 16 * 16 * 288 * 16;
constexpr unsigned SPIN_CAP = 1u << 18;
constexpr int bitrev4(int i) { return ((i & 1) << 3) | ((i & 2) << 1) | ((i & 4) >> 1) | ((i & 8) >> 3); }

constexpr int ROWP = 144;
constexpr int L_RED = 0;
constexpr int L_ML = 8192;
constexpr int L_Q = L_ML + 128;
constexpr int L_FLAG = L_Q + 512;
constexpr int L_TREF = L_FLAG + 4;
constexpr int L_MLF = L_FLAG + 16;
constexpr int L_RED2 = 9216;
constexpr int L_WQ = L_RED2;
constexpr int L_WO = L_RED2 + 32768;
constexpr int L_BYTES = L_WO + 256 * ROWP;

__device__ __forceinline__ float dot2(unsigned k, unsigned q, float acc) {
  return __builtin_amdgcn_fdot2_f32_bf16(__builtin_bit_cast(bf16x2, k), __builtin_bit_cast(bf16x2, q), acc, false);
}
__device__ __forceinline__ unsigned pk_bf16(float lo, float hi) {
  bf16x2 v; v.x = (__bf16)lo; v.y = (__bf16)hi;
  return __builtin_bit_cast(unsigned, v);
}
template <int CTRL> __device__ __forceinline__ float dpp(float x) {
  return __builtin_bit_cast(float, __builtin_amdgcn_mov_dpp(__builtin_bit_cast(int, x), CTRL, 0xf, 0xf, true));
}
template <int CTRL> __device__ __forceinline__ unsigned dppu(unsigned x) {
  return (unsigned)__builtin_amdgcn_mov_dpp((int)x, CTRL, 0xf, 0xf, true);
}
constexpr int XOR1 = 0xB1, XOR2 = 0x4E, XOR7 = 0x141, XOR8 = 0x128, BCAST = 0x150;
__device__ __forceinline__ float xrow_max(float x) {
  auto s = __builtin_amdgcn_permlane16_swap(__float_as_uint(x), __float_as_uint(x), false, false);
  x = fmaxf(__uint_as_float(s[0]), __uint_as_float(s[1]));
  auto t = __builtin_amdgcn_permlane32_swap(__float_as_uint(x), __float_as_uint(x), false, false);
  return fmaxf(__uint_as_float(t[0]), __uint_as_float(t[1]));
}
__device__ __forceinline__ float xrow_sum(float x) {
  auto s = __builtin_amdgcn_permlane16_swap(__float_as_uint(x), __float_as_uint(x), false, false);
  x = __uint_as_float(s[0]) + __uint_as_float(s[1]);
  auto t = __builtin_amdgcn_permlane32_swap(__float_as_uint(x), __float_as_uint(x), false, false);
  return __uint_as_float(t[0]) + __uint_as_float(t[1]);
}
__device__ __forceinline__ float wave_max(float x) {
  x = fmaxf(x, dpp<XOR1>(x)); x = fmaxf(x, dpp<XOR2>(x)); x = fmaxf(x, dpp<XOR7>(x)); x = fmaxf(x, dpp<XOR8>(x));
  return xrow_max(x);
}
__device__ __forceinline__ float wave_sum(float x) {
  x += dpp<XOR1>(x); x += dpp<XOR2>(x); x += dpp<XOR7>(x); x += dpp<XOR8>(x);
  return xrow_sum(x);
}
__device__ __forceinline__ f32x4 mfma16(u32x4 a, u32x4 b, f32x4 c) {
  return __builtin_amdgcn_mfma_f32_16x16x32_bf16(__builtin_bit_cast(bf16x8, a), __builtin_bit_cast(bf16x8, b), c, 0, 0, 0);
}
__device__ __forceinline__ __amdgpu_buffer_rsrc_t make_rsrc(const void* p, int bytes) {
  return __builtin_amdgcn_make_buffer_rsrc((void*)p, 0, bytes, RSRC_W3);
}
__device__ __forceinline__ u32x4 tr_frag(LAS unsigned char* p, int second) {
  s16x4 lo = __builtin_amdgcn_ds_read_tr16_b64_v4i16((LAS s16x4*)p);
  s16x4 hi = __builtin_amdgcn_ds_read_tr16_b64_v4i16((LAS s16x4*)(p + second));
  u32x2 a = __builtin_bit_cast(u32x2, lo), b = __builtin_bit_cast(u32x2, hi);
  return u32x4{a.x, a.y, b.x, b.y};
}
__device__ __forceinline__ unsigned bad_unit(u32x4 v, unsigned ta, unsigned tb) { return (v.y ^ ta) | (v.w ^ tb); }
__device__ __forceinline__ void st_unit(__amdgpu_buffer_rsrc_t r, int off, u32x4 v) { __builtin_amdgcn_raw_buffer_store_b128(v, r, off, 0, AUX_SC1); }
__device__ __forceinline__ u32x4 ld_unit(__amdgpu_buffer_rsrc_t r, int off, int soff) { return __builtin_amdgcn_raw_buffer_load_b128(r, off, soff, AUX_SC1); }
#define SB() __builtin_amdgcn_sched_barrier(0)
#define CFENCE() asm volatile("" ::: "memory")
#define BARRIER() do { CFENCE(); asm volatile("s_waitcnt lgkmcnt(0)" ::: "memory"); __builtin_amdgcn_s_barrier(); CFENCE(); SB(); } while (0)
template <int N> __device__ __forceinline__ void wait_vm() { asm volatile("s_waitcnt vmcnt(%0)" ::"n"(N) : "memory"); }
__device__ __forceinline__ void pace_until(unsigned due) {
  asm volatile("1:\n\ts_memrealtime s[96:97]\n\ts_waitcnt lgkmcnt(0)\n\ts_sub_u32 s96, s96, %0\n\ts_cmp_lt_i32 s96, 0\n\ts_cbranch_scc1 1b"
               :: "s"(due) : "s96", "s97", "scc", "memory");
}

__global__ void __launch_bounds__(NWAVES * 64, 3)
fused_kernel(const unsigned short* __restrict__ x, const unsigned short* __restrict__ kc, const unsigned short* __restrict__ vc,
             const unsigned short* __restrict__ wq, const unsigned short* __restrict__ wo, unsigned short* __restrict__ out,
             unsigned char* __restrict__ ws) {
  __shared__ __attribute__((aligned(16))) unsigned char smem[L_BYTES];
  LAS unsigned char* L = (LAS unsigned char*)smem;
  const int bid = blockIdx.x;
  const int kvh = (bid & 7) * 2 + ((bid >> 3) & 1), b = bid >> 4;
  const int tid = threadIdx.x, l = tid & 63, w = __builtin_amdgcn_readfirstlane(tid >> 6), g = l >> 4, c = l & 15;
  const int sw = w - 1;
  unsigned pf;
  {
    unsigned long long pc; unsigned clen;
    asm volatile("s_getpc_b64 %0\n.Lfused_code_begin:\n\ts_mov_b32 %1, .Lfused_code_end-.Lfused_code_begin" : "=s"(pc), "=s"(clen));
    pf = __builtin_amdgcn_raw_buffer_load_b32(make_rsrc((const void*)pc, (int)clen), tid < 8 ? (tid * 32 + (bid >> 3)) * 128 : 0x7fff0000, 0, 0);
  }
  unsigned long long t64 = __builtin_amdgcn_s_memrealtime();
  *(LAS unsigned*)(L + L_FLAG + (l & 15) * 4) = 0u;
  const bool oproj = b < 9;
  const int offa_s = ((b * HKV + kvh) * SK + (SK - W) - 4) * (DH * 2) + tid * 16, offa_q = (l >> 1) * (NQ * 2) + (l & 1) * 16 + (kvh * 256 + b * 16) * 2;
  const int offb_s = (kvh * 256 - 32 + (tid >> 6) * 32 + (l >> 3)) * (D * 2) + (oproj ? b * 128 : 0x7f000000) + (l & 7) * 16, offb_q = c * (D * 2) + g * 16;
  int offa = tid < 64 ? offa_q : offa_s, offb = tid < 64 ? offb_q : offb_s;
  const int rot8k = (bid >> 3) * 8192;
  asm volatile("" : "+v"(offa), "+v"(offb));
  SB();
  asm volatile("" : "+s"(t64) : "s"(x), "s"(kc), "s"(vc), "s"(wq), "s"(wo), "s"(out), "s"(ws));
  const unsigned t0 = (unsigned)t64;
  const __amdgpu_buffer_rsrc_t rws = make_rsrc(ws, WS_END);
  const int l16 = l * 16;
  unsigned long long e1;
  unsigned ta, tb;
#define EPOCH_GET() do { asm volatile("s_load_dwordx2 %0, %1, 0x0\n\ts_waitcnt lgkmcnt(0)" : "=s"(e1) : "s"(ws + WS_EPOCH) : "memory"); SB(); \
    e1 += 1ull; ta = (unsigned)e1; tb = (unsigned)(e1 >> 32) ^ (ta * 0x9E3779B1u); } while (0)

  if (w == 0) {
    __builtin_amdgcn_s_setprio(3);
    {
      const __amdgpu_buffer_rsrc_t rwq = make_rsrc(wq, D * NQ * 2);
      const __amdgpu_buffer_rsrc_t rx = make_rsrc(x, B * D * 2);
      u32x4 wr[18], xr[18];
#pragma unroll
      for (int s = 0; s < 18; ++s) {
        wr[s] = __builtin_amdgcn_raw_buffer_load_b128(rwq, offa, s * 32 * NQ * 2, AUX_NT); SB();
        xr[s] = __builtin_amdgcn_raw_buffer_load_b128(rx, offb + s * 64, 0, 0); SB();
        if (s == 2) BARRIER();
      }
      EPOCH_GET();
      if (bid < 8 && l < 8) st_unit(rws, WS_TBOX + bid * 128 + l16, u32x4{t0, ta, t0, tb});
#pragma unroll
      for (int s = 0; s < 18; ++s) *(LAS u32x4*)(L + L_WQ + s * 1024 + l16) = wr[s];
      f32x4 acc = {0.f, 0.f, 0.f, 0.f};
      const int troff = (8 * g + (c >> 2)) * 32 + (c & 3) * 8;
#pragma unroll
      for (int s = 0; s < 18; ++s) acc = mfma16(tr_frag(L + L_WQ + s * 1024 + troff, 128), xr[s], acc);
      st_unit(rws, WS_QBOX + kvh * 16384 + c * 1024 + (b * 4 + g) * 16, u32x4{pk_bf16(acc[0], acc[1]), ta, pk_bf16(acc[2], acc[3]), tb});
    }
    asm volatile("" ::"v"(pf));
    while ((unsigned)__builtin_amdgcn_s_memrealtime() - t0 < (unsigned)P_TPOLL) __builtin_amdgcn_s_sleep(4);
    {
      const u32x4 tu = ld_unit(rws, (l & 7) * 128, WS_TBOX);
      u32x4 qa = ld_unit(rws, l16, WS_QBOX + (kvh * 16 + b) * 1024);
      int d = bad_unit(tu, ta, tb) == 0u ? (int)(tu.x - t0) : 0;
      d = min(max(d, -300), 300);
      d += __builtin_amdgcn_mov_dpp(d, XOR1, 0xf, 0xf, true); d += __builtin_amdgcn_mov_dpp(d, XOR2, 0xf, 0xf, true);
      d += __builtin_amdgcn_mov_dpp(d, XOR7, 0xf, 0xf, true); d >>= 3;
      *(LAS unsigned*)(L + L_TREF) = t0 + (unsigned)d;
      for (unsigned spins = 0; !__all(bad_unit(qa, ta, tb) == 0u) && spins < SPIN_CAP; ++spins) {
        __builtin_amdgcn_s_sleep(8);
        CFENCE();
        qa = ld_unit(rws, l16, WS_QBOX + (kvh * 16 + b) * 1024);
      }
      *(LAS u32x2*)(L + L_Q + l * 8) = u32x2{qa.x, qa.z};
      asm volatile("s_waitcnt lgkmcnt(0)" ::: "memory");
      *(volatile LAS unsigned*)(L + L_FLAG) = 1u;
    }
    __builtin_amdgcn_s_setprio(0);
    for (unsigned spins = 0; !__all(*(volatile LAS unsigned*)(L + L_MLF + (l & 7) * 4) != 0u) && spins < (SPIN_CAP << 4); ++spins) __builtin_amdgcn_s_sleep(16);
    CFENCE();
    float fw[8];
    {
      const int h = l >> 5;
      float mw[8], lw[8];
#pragma unroll
      for (int ww = 0; ww < 8; ++ww) { mw[ww] = *(LAS float*)(L + L_ML + ww * 16 + h * 4); lw[ww] = *(LAS float*)(L + L_ML + ww * 16 + 8 + h * 4); }
      float M = mw[0];
#pragma unroll
      for (int ww = 1; ww < 8; ++ww) M = fmaxf(M, mw[ww]);
      float Ls = 0.f;
#pragma unroll
      for (int ww = 0; ww < 8; ++ww) { fw[ww] = __builtin_amdgcn_exp2f(mw[ww] - M); Ls = fmaf(fw[ww], lw[ww], Ls); }
      const float inv = 1.0f / Ls;
#pragma unroll
      for (int ww = 0; ww < 8; ++ww) { fw[ww] *= inv; asm volatile("" : "+v"(fw[ww])); }
    }
    int ooff = WS_OBOX + (kvh * 16 + b) * 1024 + (l >> 3) * 128 + (l & 1) * 64 + ((l >> 1) & 3) * 16;
    asm volatile("" : "+v"(ooff));
    __builtin_amdgcn_s_setprio(3);
    BARRIER();
    f32x4 ow[8];
#pragma unroll
    for (int ww = 0; ww < 8; ++ww) ow[ww] = *(LAS f32x4*)(L + L_RED + ww * 1024 + l16);
    const f32x4 O = ((fw[0] * ow[0] + fw[1] * ow[1]) + (fw[2] * ow[2] + fw[3] * ow[3])) + ((fw[4] * ow[4] + fw[5] * ow[5]) + (fw[6] * ow[6] + fw[7] * ow[7]));
    st_unit(rws, ooff, u32x4{pk_bf16(O.x, O.y), ta, pk_bf16(O.z, O.w), tb});
    if (oproj) BARRIER();
  } else {
    const __amdgpu_buffer_rsrc_t rk = make_rsrc(kc, B * HKV * SK * DH * 2);
    const __amdgpu_buffer_rsrc_t rv = make_rsrc(vc, B * HKV * SK * DH * 2);
    unsigned tp0 = t0 + (unsigned)(P_PACE0 + ((sw * P_PACE) >> 5));
    u32x4 R[64];
    const __amdgpu_buffer_rsrc_t rwo = make_rsrc(wo, NQ * D * 2);
    u32x4 wor[4];
    constexpr auto item = [](int j) constexpr { return j == 0 ? 0 : (j == 1 ? 32 : (j <= 32 ? j - 1 : j)); };
    constexpr auto pos = [](int i) constexpr { return i == 0 ? 0 : (i == 32 ? 1 : (i < 32 ? i + 1 : i)); };
#define ISSUE(j) do { constexpr int it_ = item(j);                                                                               \
      if constexpr ((j) >= W0) { wait_vm<WIN - 1>(); pace_until(tp0 + (unsigned)(((j) * P_PACE) >> 2)); SB(); }                  \
      R[it_] = __builtin_amdgcn_raw_buffer_load_b128(it_ < 32 ? rk : rv, offa, (it_ * 8192 + rot8k) & 0x3ffff, AUX_NT); SB(); } while (0)
    auto phase_a = [&](auto self, auto jc) {
      constexpr int j = decltype(jc)::value;
      if constexpr (j < KB) {
        ISSUE(j);
        if constexpr (j == 1) {
#pragma unroll
          for (int u = 0; u < 4; ++u) { wor[u] = __builtin_amdgcn_raw_buffer_load_b128(rwo, offb, u * 8 * D * 2, AUX_NT); SB(); }
        }
        if constexpr (j == W0 - 1) { BARRIER(); EPOCH_GET(); }
        if constexpr (j == W0) {
#pragma unroll
          for (int u = 0; u < 4; ++u) *(LAS u32x4*)(L + L_WO + (sw * 32 + u * 8 + (l >> 3)) * ROWP + (l & 7) * 16) = wor[u];
          SB();
        }
        self(self, std::integral_constant<int, j + 1>{});
      }
    };
    phase_a(phase_a, std::integral_constant<int, 0>{});
    asm volatile("" ::"v"(pf));
    for (unsigned spins = 0; *(volatile LAS unsigned*)(L + L_FLAG) == 0u && spins < (SPIN_CAP << 4); ++spins) __builtin_amdgcn_s_sleep(1);
    CFENCE();
    {
      const int dt = (int)((unsigned)__builtin_amdgcn_readfirstlane(*(LAS unsigned*)(L + L_TREF)) - t0);
      tp0 = t0 + (unsigned)(min(max(dt, -400), 400) + P_PACE1 + ((sw * P_PACE) >> 5));
    }
    const u32x4 q0 = *(LAS u32x4*)(L + L_Q + c * 16), q1 = *(LAS u32x4*)(L + L_Q + 256 + c * 16);
    const bool up8 = (c & 8) != 0, up4 = (c & 4) != 0, up2 = (c & 2) != 0, up1 = (c & 1) != 0;
    float u0[4], u1[4], sc[2][2];
    const int hh = c >> 3, jj = c & 7;
    const unsigned pick = (unsigned)((2 * hh + 1) << 8 | (2 * hh));
    const unsigned act = (jj & 1) ? (pick << 16 | 0x0C0Cu) : (0x0C0C0000u | pick);
    u32x4 sel;
    sel.x = (jj >> 1) == 0 ? act : 0x0C0C0C0Cu; sel.y = (jj >> 1) == 1 ? act : 0x0C0C0C0Cu;
    sel.z = (jj >> 1) == 2 ? act : 0x0C0C0C0Cu; sel.w = (jj >> 1) == 3 ? act : 0x0C0C0C0Cu;
    unsigned P0 = 0, P1 = 0;
    f32x4 acc = {0.f, 0.f, 0.f, 0.f};
    u32x4 afr = {0u, 0u, 0u, 0u};
    constexpr float C2 = 0.088388347648318440f * 1.4426950408889634f;
#define COMB(UP, CTRL, LO, HI) ((UP ? HI : LO) + dpp<CTRL>(UP ? LO : HI))
#define AFRAG(I) do { const unsigned t_ = dppu<BCAST + bitrev4((I) & 15)>((I) < 16 ? P0 : P1);                                   \
      afr.x = __builtin_amdgcn_perm(t_, t_, sel.x); afr.y = __builtin_amdgcn_perm(t_, t_, sel.y);                                \
      afr.z = __builtin_amdgcn_perm(t_, t_, sel.z); afr.w = __builtin_amdgcn_perm(t_, t_, sel.w); } while (0)
    auto process = [&](auto ic) {
      constexpr int i = decltype(ic)::value;
      if constexpr (i < 32) {
        const u32x4 kk = R[i];
        float a = dot2(kk.x, q0.x, 0.f), bb = dot2(kk.x, q1.x, 0.f);
        a = dot2(kk.y, q0.y, a); bb = dot2(kk.y, q1.y, bb);
        a = dot2(kk.z, q0.z, a); bb = dot2(kk.z, q1.z, bb);
        a = dot2(kk.w, q0.w, a); bb = dot2(kk.w, q1.w, bb);
        if constexpr (!(i & 1)) { u0[0] = a; u1[0] = bb; }
        else {
          a = COMB(up8, XOR8, u0[0], a); bb = COMB(up8, XOR8, u1[0], bb);
          if constexpr (!(i & 2)) { u0[1] = a; u1[1] = bb; }
          else {
            a = COMB(up4, XOR7, u0[1], a); bb = COMB(up4, XOR7, u1[1], bb);
            if constexpr (!(i & 4)) { u0[2] = a; u1[2] = bb; }
            else {
              a = COMB(up2, XOR2, u0[2], a); bb = COMB(up2, XOR2, u1[2], bb);
              if constexpr (!(i & 8)) { u0[3] = a; u1[3] = bb; }
              else {
                a = COMB(up1, XOR1, u0[3], a); bb = COMB(up1, XOR1, u1[3], bb);
                sc[i >> 4][0] = a; sc[i >> 4][1] = bb;
              }
            }
          }
        }
        if constexpr (i == 31) {
          float m[2], lsum[2], p[2][2];
#pragma unroll
          for (int h = 0; h < 2; ++h) {
            const float s0 = sc[0][h] * C2, s1 = sc[1][h] * C2;
            m[h] = wave_max(fmaxf(s0, s1));
            p[0][h] = __builtin_amdgcn_exp2f(s0 - m[h]); p[1][h] = __builtin_amdgcn_exp2f(s1 - m[h]);
            lsum[h] = wave_sum(p[0][h] + p[1][h]);
          }
          P0 = pk_bf16(p[0][0], p[0][1]); P1 = pk_bf16(p[1][0], p[1][1]);
          if (l == 0) {
            *(LAS f32x4*)(L + L_ML + sw * 16) = f32x4{m[0], m[1], lsum[0], lsum[1]};
            CFENCE();
            *(volatile LAS unsigned*)(L + L_MLF + sw * 4) = 1u;
          }
          AFRAG(0);
        }
      } else {
        acc = mfma16(afr, R[i], acc);
        if constexpr (i < 63) AFRAG(i - 31);
      }
      SB();
    };
    auto phase_b = [&](auto self, auto jc, auto dc) {
      constexpr int j = decltype(jc)::value, done = decltype(dc)::value;
      if constexpr (done < 64) {
        constexpr int landed = j < 64 ? j - WIN : 64;
        auto run = [&](auto self2, auto ic) {
          constexpr int i = decltype(ic)::value;
          if constexpr (i < 64 && i < done + CHUNK && pos(i < 64 ? i : 0) < landed) { process(ic); return self2(self2, std::integral_constant<int, i + 1>{}); }
          else return ic;
        };
        auto nd = run(run, std::integral_constant<int, done>{});
        if constexpr (j < 64) ISSUE(j);
        self(self, std::integral_constant<int, (j < 64 ? j + 1 : 64)>{}, nd);
      }
    };
    phase_b(phase_b, std::integral_constant<int, KB>{}, std::integral_constant<int, 0>{});
#undef COMB
#undef AFRAG
#undef ISSUE
    *(LAS f32x4*)(L + L_RED + sw * 1024 + (g >> 1) * 512 + c * 32 + (g & 1) * 16) = acc;
    BARRIER();
    if (oproj) {
      const int off = c * 1024 + sw * 128 + g * 16;
      __builtin_amdgcn_s_sleep(P_D1);
      SB();
      u32x4 a0 = ld_unit(rws, off, WS_OBOX + kvh * 16384), a1 = ld_unit(rws, off + 64, WS_OBOX + kvh * 16384);
      SB();
      u32x4 wfr[4];
#pragma unroll
      for (int t = 0; t < 4; ++t) wfr[t] = tr_frag(L + L_WO + (sw * 32 + 8 * g + (c >> 2)) * ROWP + t * 32 + (c & 3) * 8, 4 * ROWP);
      SB();
      {
        bool ok = (bad_unit(a0, ta, tb) | bad_unit(a1, ta, tb)) == 0u;
        for (unsigned spins = 0; !__all(ok) && spins < SPIN_CAP; ++spins) {
          CFENCE();
          if (!ok) {
            a0 = ld_unit(rws, off, WS_OBOX + kvh * 16384);
            a1 = ld_unit(rws, off + 64, WS_OBOX + kvh * 16384);
            ok = (bad_unit(a0, ta, tb) | bad_unit(a1, ta, tb)) == 0u;
          }
        }
      }
      const u32x4 of = {a0.x, a0.z, a1.x, a1.z};
#pragma unroll
      for (int t = 0; t < 4; ++t) {
        const f32x4 d = mfma16(wfr[t], of, f32x4{0.f, 0.f, 0.f, 0.f});
        *(LAS f32x4*)(L + L_RED2 + (sw * 4 + t) * 1024 + l16) = d;
      }
      BARRIER();
      if (sw < 4) {
        f32x4 s = *(LAS f32x4*)(L + L_RED2 + sw * 1024 + l16);
#pragma unroll
        for (int ww = 1; ww < 8; ++ww) s += *(LAS f32x4*)(L + L_RED2 + (ww * 4 + sw) * 1024 + l16);
        const bool lo = c < 8;
        const unsigned v0 = __float_as_uint(s.x), v1 = __float_as_uint(s.y), n0 = dppu<XOR8>(__float_as_uint(s.z)), n1 = dppu<XOR8>(__float_as_uint(s.w));
        const int base = WS_PBOX + (((c & 7) * 16 + kvh) * 288 + b * 32 + sw * 8 + g * 2) * 16;
        st_unit(rws, base + (lo ? 0 : 16), u32x4{lo ? v0 : n0, ta, lo ? v1 : n1, tb});
        st_unit(rws, base + 8 * 16 * 288 * 16 + (lo ? 16 : 0), u32x4{lo ? n0 : v0, ta, lo ? n1 : v1, tb});
      } else {
        const int poff = ((kvh * 16 + (l >> 3)) * 288 + b * 32 + (sw - 4) * 8 + (l & 7)) * 16;
        __builtin_amdgcn_s_sleep(P_D2);
        u32x4 p0 = ld_unit(rws, poff, WS_PBOX), p1 = ld_unit(rws, poff + 8 * 288 * 16, WS_PBOX);
        bool ok0 = bad_unit(p0, ta, tb) == 0u, ok1 = bad_unit(p1, ta, tb) == 0u;
        for (unsigned spins = 0; !__all(ok0 && ok1) && spins < SPIN_CAP; ++spins) {
          CFENCE();
          if (!ok0) p0 = ld_unit(rws, poff, WS_PBOX);
          if (!ok1) p1 = ld_unit(rws, poff + 8 * 288 * 16, WS_PBOX);
          ok0 = bad_unit(p0, ta, tb) == 0u; ok1 = bad_unit(p1, ta, tb) == 0u;
        }
        float v0 = __uint_as_float(p0.x) + __uint_as_float(p1.x), v1 = __uint_as_float(p0.z) + __uint_as_float(p1.z);
        v0 += dpp<XOR8>(v0); v1 += dpp<XOR8>(v1);
        v0 = xrow_sum(v0); v1 = xrow_sum(v1);
        if (l < 8) *(unsigned*)(out + kvh * D + b * 64 + (sw - 4) * 16 + l * 2) = pk_bf16(v0, v1);
        if (bid == 0 && tid == 5 * 64) __builtin_amdgcn_raw_buffer_store_b64(u32x2{ta, (unsigned)(e1 >> 32)}, rws, 0, WS_EPOCH, AUX_SC1);
      }
    }
  }
  asm volatile(".Lfused_code_end:");
}

extern "C" void kernel_launch(void* const* d_in, const int* in_sizes, int n_in, void* d_out, int out_size, void* d_ws,
                              size_t ws_size, hipStream_t stream) {
  fused_kernel<<<256, NWAVES * 64, 0, stream>>>((const unsigned short*)d_in[0], (const unsigned short*)d_in[1], (const unsigned short*)d_in[2],
                                                (const unsigned short*)d_in[3], (const unsigned short*)d_in[4], (unsigned short*)d_out,
                                                (unsigned char*)d_ws);
}
```

```cpp
#include <hip/hip_runtime.h>
#include <hip/hip_bf16.h>
#include <cstdint>
#include <type_traits>

#define DIAG 0
#define P_PACE 120
#define P_PACE0 150
#define P_PACE1 125
#define P_TPOLL 660
#define P_D1 14
#define P_D2 25

constexpr int B = 16, D = 576, HQ = 32, HKV = 16, SK = 4096, DH = 128, W = 1024;
constexpr int NQ = HQ * DH;
constexpr int NWAVES = 9;
constexpr int W0 = 4;
constexpr int WIN = 6;
constexpr int KB = 30;
constexpr int CHUNK = 3;

typedef unsigned u32x4 __attribute__((ext_vector_type(4)));
typedef unsigned u32x2 __attribute__((ext_vector_type(2)));
typedef float f32x4 __attribute__((ext_vector_type(4)));
typedef short bf16x8 __attribute__((ext_vector_type(8)));
typedef short s16x4 __attribute__((ext_vector_type(4)));
typedef __bf16 bf16x2 __attribute__((ext_vector_type(2)));
#define LAS __attribute__((address_space(3)))

constexpr int AUX_SC1 = 16, AUX_NT = 2;
constexpr int RSRC_W3 = 0x00020000;

constexpr int WS_EPOCH = 0;
constexpr int WS_TBOX = 1024;
constexpr int WS_QBOX = 4096;
constexpr int WS_OBOX = WS_QBOX + 16 * 16 * 1024;
constexpr int WS_PBOX = WS_OBOX + 16 * 16 * 1024;
constexpr int WS_END = WS_PBOX + 16 * 16 * 288 * 16;
constexpr int WS_DIAG = 8 << 20;
constexpr unsigned SPIN_CAP = 1u << 18;
constexpr int bitrev4(int i) { return ((i & 1) << 3) | ((i & 2) << 1) | ((i & 4) >> 1) | ((i & 8) >> 3); }

constexpr int ROWP = 144;
constexpr int L_RED = 0;
constexpr int L_ML = 8192;
constexpr int L_Q = L_ML + 128;
constexpr int L_FLAG = L_Q + 512;
constexpr int L_TREF = L_FLAG + 4;
constexpr int L_MLF = L_FLAG + 16;
constexpr int L_RED2 = 9216;
constexpr int L_WQ = L_RED2;
constexpr int L_WO = L_RED2 + 32768;
constexpr int L_BYTES = L_WO + 256 * ROWP;

__device__ __forceinline__ float dot2(unsigned k, unsigned q, float acc) {
  return __builtin_amdgcn_fdot2_f32_bf16(__builtin_bit_cast(bf16x2, k), __builtin_bit_cast(bf16x2, q), acc, false);
}
__device__ __forceinline__ unsigned pk_bf16(float lo, float hi) {
  bf16x2 v; v.x = (__bf16)lo; v.y = (__bf16)hi;
  return __builtin_bit_cast(unsigned, v);
}
template <int CTRL> __device__ __forceinline__ float dpp(float x) {
  return __builtin_bit_cast(float, __builtin_amdgcn_mov_dpp(__builtin_bit_cast(int, x), CTRL, 0xf, 0xf, true));
}
template <int CTRL> __device__ __forceinline__ unsigned dppu(unsigned x) {
  return (unsigned)__builtin_amdgcn_mov_dpp((int)x, CTRL, 0xf, 0xf, true);
}
constexpr int XOR1 = 0xB1, XOR2 = 0x4E, XOR7 = 0x141, XOR8 = 0x128, BCAST = 0x150;
__device__ __forceinline__ float xrow_max(float x) {
  auto s = __builtin_amdgcn_permlane16_swap(__float_as_uint(x), __float_as_uint(x), false, false);
  x = fmaxf(__uint_as_float(s[0]), __uint_as_float(s[1]));
  auto t = __builtin_amdgcn_permlane32_swap(__float_as_uint(x), __float_as_uint(x), false, false);
  return fmaxf(__uint_as_float(t[0]), __uint_as_float(t[1]));
}
__device__ __forceinline__ float xrow_sum(float x) {
  auto s = __builtin_amdgcn_permlane16_swap(__float_as_uint(x), __float_as_uint(x), false, false);
  x = __uint_as_float(s[0]) + __uint_as_float(s[1]);
  auto t = __builtin_amdgcn_permlane32_swap(__float_as_uint(x), __float_as_uint(x), false, false);
  return __uint_as_float(t[0]) + __uint_as_float(t[1]);
}
__device__ __forceinline__ float wave_max(float x) {
  x = fmaxf(x, dpp<XOR1>(x)); x = fmaxf(x, dpp<XOR2>(x)); x = fmaxf(x, dpp<XOR7>(x)); x = fmaxf(x, dpp<XOR8>(x));
  return xrow_max(x);
}
__device__ __forceinline__ float wave_sum(float x) {
  x += dpp<XOR1>(x); x += dpp<XOR2>(x); x += dpp<XOR7>(x); x += dpp<XOR8>(x);
  return xrow_sum(x);
}
__device__ __forceinline__ f32x4 mfma16(u32x4 a, u32x4 b, f32x4 c) {
  return __builtin_amdgcn_mfma_f32_16x16x32_bf16(__builtin_bit_cast(bf16x8, a), __builtin_bit_cast(bf16x8, b), c, 0, 0, 0);
}
__device__ __forceinline__ __amdgpu_buffer_rsrc_t make_rsrc(const void* p, int bytes) {
  return __builtin_amdgcn_make_buffer_rsrc((void*)p, 0, bytes, RSRC_W3);
}
__device__ __forceinline__ u32x4 tr_frag(LAS unsigned char* p, int second) {
  s16x4 lo = __builtin_amdgcn_ds_read_tr16_b64_v4i16((LAS s16x4*)p);
  s16x4 hi = __builtin_amdgcn_ds_read_tr16_b64_v4i16((LAS s16x4*)(p + second));
  u32x2 a = __builtin_bit_cast(u32x2, lo), b = __builtin_bit_cast(u32x2, hi);
  return u32x4{a.x, a.y, b.x, b.y};
}
__device__ __forceinline__ unsigned bad_unit(u32x4 v, unsigned ta, unsigned tb) { return (v.y ^ ta) | (v.w ^ tb); }
__device__ __forceinline__ void st_unit(__amdgpu_buffer_rsrc_t r, int off, u32x4 v) { __builtin_amdgcn_raw_buffer_store_b128(v, r, off, 0, AUX_SC1); }
__device__ __forceinline__ u32x4 ld_unit(__amdgpu_buffer_rsrc_t r, int off, int soff) { return __builtin_amdgcn_raw_buffer_load_b128(r, off, soff, AUX_SC1); }
#define SB() __builtin_amdgcn_sched_barrier(0)
#define CFENCE() asm volatile("" ::: "memory")
#define BARRIER() do { CFENCE(); asm volatile("s_waitcnt lgkmcnt(0)" ::: "memory"); __builtin_amdgcn_s_barrier(); CFENCE(); SB(); } while (0)
template <int N> __device__ __forceinline__ void wait_vm() { asm volatile("s_waitcnt vmcnt(%0)" ::"n"(N) : "memory"); }
__device__ __forceinline__ void pace_until(unsigned due) {
  asm volatile("1:\n\ts_memrealtime s[96:97]\n\ts_waitcnt lgkmcnt(0)\n\ts_sub_u32 s96, s96, %0\n\ts_cmp_lt_i32 s96, 0\n\ts_cbranch_scc1 1b"
               :: "s"(due) : "s96", "s97", "scc", "memory");
}

#define STAMP(i) do { } while (0)

__global__ void __launch_bounds__(NWAVES * 64, 3)
fused_kernel(const unsigned short* __restrict__ x, const unsigned short* __restrict__ kc, const unsigned short* __restrict__ vc,
             const unsigned short* __restrict__ wq, const unsigned short* __restrict__ wo, unsigned short* __restrict__ out,
             unsigned char* __restrict__ ws) {
  __shared__ __attribute__((aligned(16))) unsigned char smem[L_BYTES];
  LAS unsigned char* L = (LAS unsigned char*)smem;
  const int bid = blockIdx.x;
  const int kvh = (bid & 7) * 2 + ((bid >> 3) & 1), b = bid >> 4;
  const int tid = threadIdx.x, l = tid & 63, w = __builtin_amdgcn_readfirstlane(tid >> 6), g = l >> 4, c = l & 15;
  const int sw = w - 1;
  STAMP(0);
  unsigned pf;
  {
    unsigned long long pc; unsigned clen;
    asm volatile("s_getpc_b64 %0\n.Lfused_code_begin:\n\ts_mov_b32 %1, .Lfused_code_end-.Lfused_code_begin" : "=s"(pc), "=s"(clen));
    pf = __builtin_amdgcn_raw_buffer_load_b32(make_rsrc((const void*)pc, (int)clen), tid < 8 ? (tid * 32 + (bid >> 3)) * 128 : 0x7fff0000, 0, 0);
  }
  unsigned long long t64 = __builtin_amdgcn_s_memrealtime();
  *(LAS unsigned*)(L + L_FLAG + (l & 15) * 4) = 0u;
  const bool oproj = b < 9;
  const int offa_s = ((b * HKV + kvh) * SK + (SK - W) - 4) * (DH * 2) + tid * 16, offa_q = (l >> 1) * (NQ * 2) + (l & 1) * 16 + (kvh * 256 + b * 16) * 2;
  const int offb_s = (kvh * 256 - 32 + (tid >> 6) * 32 + (l >> 3)) * (D * 2) + (oproj ? b * 128 : 0x7f000000) + (l & 7) * 16, offb_q = c * (D * 2) + g * 16;
  int offa = tid < 64 ? offa_q : offa_s, offb = tid < 64 ? offb_q : offb_s;
  const int rot8k = (bid >> 3) * 8192;
  asm volatile("" : "+v"(offa), "+v"(offb));
  SB();
  asm volatile("" : "+s"(t64) : "s"(x), "s"(kc), "s"(vc), "s"(wq), "s"(wo), "s"(out), "s"(ws));
  const unsigned t0 = (unsigned)t64;
  const __amdgpu_buffer_rsrc_t rws = make_rsrc(ws, WS_END);
  const int l16 = l * 16;
  unsigned long long e1;
  unsigned ta, tb;
#define EPOCH_GET() do { asm volatile("s_load_dwordx2 %0, %1, 0x0\n\ts_waitcnt lgkmcnt(0)" : "=s"(e1) : "s"(ws + WS_EPOCH) : "memory"); SB(); \
    e1 += 1ull; ta = (unsigned)e1; tb = (unsigned)(e1 >> 32) ^ (ta * 0x9E3779B1u); } while (0)

  if (w == 0) {
    __builtin_amdgcn_s_setprio(3);
    {
      const __amdgpu_buffer_rsrc_t rwq = make_rsrc(wq, D * NQ * 2);
      const __amdgpu_buffer_rsrc_t rx = make_rsrc(x, B * D * 2);
      u32x4 wr[18], xr[18];
#pragma unroll
      for (int s = 0; s < 18; ++s) {
        wr[s] = __builtin_amdgcn_raw_buffer_load_b128(rwq, offa, s * 32 * NQ * 2, AUX_NT); SB();
        xr[s] = __builtin_amdgcn_raw_buffer_load_b128(rx, offb + s * 64, 0, 0); SB();
        if (s == 2) BARRIER();
      }
      EPOCH_GET();
      if (bid < 8 && l < 8) st_unit(rws, WS_TBOX + bid * 128 + l16, u32x4{t0, ta, t0, tb});
      STAMP(1);
#pragma unroll
      for (int s = 0; s < 18; ++s) *(LAS u32x4*)(L + L_WQ + s * 1024 + l16) = wr[s];
      f32x4 acc = {0.f, 0.f, 0.f, 0.f};
      const int troff = (8 * g + (c >> 2)) * 32 + (c & 3) * 8;
#pragma unroll
      for (int s = 0; s < 18; ++s) acc = mfma16(tr_frag(L + L_WQ + s * 1024 + troff, 128), xr[s], acc);
      STAMP(2);
      st_unit(rws, WS_QBOX + kvh * 16384 + c * 1024 + (b * 4 + g) * 16, u32x4{pk_bf16(acc[0], acc[1]), ta, pk_bf16(acc[2], acc[3]), tb});
      STAMP(3);
    }
    asm volatile("" ::"v"(pf));
    while ((unsigned)__builtin_amdgcn_s_memrealtime() - t0 < (unsigned)P_TPOLL) __builtin_amdgcn_s_sleep(4);
    {
      const u32x4 tu = ld_unit(rws, (l & 7) * 128, WS_TBOX);
      u32x4 qa = ld_unit(rws, l16, WS_QBOX + (kvh * 16 + b) * 1024);
      int d = bad_unit(tu, ta, tb) == 0u ? (int)(tu.x - t0) : 0;
      d = min(max(d, -300), 300);
      d += __builtin_amdgcn_mov_dpp(d, XOR1, 0xf, 0xf, true); d += __builtin_amdgcn_mov_dpp(d, XOR2, 0xf, 0xf, true);
      d += __builtin_amdgcn_mov_dpp(d, XOR7, 0xf, 0xf, true); d >>= 3;
      *(LAS unsigned*)(L + L_TREF) = t0 + (unsigned)d;
      for (unsigned spins = 0; !__all(bad_unit(qa, ta, tb) == 0u) && spins < SPIN_CAP; ++spins) {
        __builtin_amdgcn_s_sleep(8);
        CFENCE();
        qa = ld_unit(rws, l16, WS_QBOX + (kvh * 16 + b) * 1024);
      }
      *(LAS u32x2*)(L + L_Q + l * 8) = u32x2{qa.x, qa.z};
      asm volatile("s_waitcnt lgkmcnt(0)" ::: "memory");
      *(volatile LAS unsigned*)(L + L_FLAG) = 1u;
    }
    STAMP(4);
    __builtin_amdgcn_s_setprio(0);
    for (unsigned spins = 0; !__all(*(volatile LAS unsigned*)(L + L_MLF + (l & 7) * 4) != 0u) && spins < (SPIN_CAP << 4); ++spins) __builtin_amdgcn_s_sleep(16);
    CFENCE();
    float fw[8];
    {
      const int h = l >> 5;
      float mw[8], lw[8];
#pragma unroll
      for (int ww = 0; ww < 8; ++ww) { mw[ww] = *(LAS float*)(L + L_ML + ww * 16 + h * 4); lw[ww] = *(LAS float*)(L + L_ML + ww * 16 + 8 + h * 4); }
      float M = mw[0];
#pragma unroll
      for (int ww = 1; ww < 8; ++ww) M = fmaxf(M, mw[ww]);
      float Ls = 0.f;
#pragma unroll
      for (int ww = 0; ww < 8; ++ww) { fw[ww] = __builtin_amdgcn_exp2f(mw[ww] - M); Ls = fmaf(fw[ww], lw[ww], Ls); }
      const float inv = 1.0f / Ls;
#pragma unroll
      for (int ww = 0; ww < 8; ++ww) { fw[ww] *= inv; asm volatile("" : "+v"(fw[ww])); }
    }
    int ooff = WS_OBOX + (kvh * 16 + b) * 1024 + (l >> 3) * 128 + (l & 1) * 64 + ((l >> 1) & 3) * 16;
    asm volatile("" : "+v"(ooff));
    __builtin_amdgcn_s_setprio(3);
    BARRIER();
    STAMP(6);
    f32x4 ow[8];
#pragma unroll
    for (int ww = 0; ww < 8; ++ww) ow[ww] = *(LAS f32x4*)(L + L_RED + ww * 1024 + l16);
    const f32x4 O = ((fw[0] * ow[0] + fw[1] * ow[1]) + (fw[2] * ow[2] + fw[3] * ow[3])) + ((fw[4] * ow[4] + fw[5] * ow[5]) + (fw[6] * ow[6] + fw[7] * ow[7]));
    st_unit(rws, ooff, u32x4{pk_bf16(O.x, O.y), ta, pk_bf16(O.z, O.w), tb});
    STAMP(7);
    if (oproj) BARRIER();
  } else {
    const __amdgpu_buffer_rsrc_t rk = make_rsrc(kc, B * HKV * SK * DH * 2);
    const __amdgpu_buffer_rsrc_t rv = make_rsrc(vc, B * HKV * SK * DH * 2);
    unsigned tp0 = t0 + (unsigned)(P_PACE0 + ((sw * P_PACE) >> 5));
    u32x4 R[64];
    const __amdgpu_buffer_rsrc_t rwo = make_rsrc(wo, NQ * D * 2);
    u32x4 wor[4];
    constexpr auto item = [](int j) constexpr { return j == 0 ? 0 : (j == 1 ? 32 : (j <= 32 ? j - 1 : j)); };
    constexpr auto pos = [](int i) constexpr { return i == 0 ? 0 : (i == 32 ? 1 : (i < 32 ? i + 1 : i)); };
#define ISSUE(j) do { constexpr int it_ = item(j);                                                                               \
      if constexpr ((j) >= W0) { wait_vm<WIN - 1>(); pace_until(tp0 + (unsigned)(((j) * P_PACE) >> 2)); SB(); }                  \
      R[it_] = __builtin_amdgcn_raw_buffer_load_b128(it_ < 32 ? rk : rv, offa, (it_ * 8192 + rot8k) & 0x3ffff, AUX_NT); SB(); } while (0)
    auto phase_a = [&](auto self, auto jc) {
      constexpr int j = decltype(jc)::value;
      if constexpr (j < KB) {
        ISSUE(j);
        if constexpr (j == 1) {
#pragma unroll
          for (int u = 0; u < 4; ++u) { wor[u] = __builtin_amdgcn_raw_buffer_load_b128(rwo, offb, u * 8 * D * 2, AUX_NT); SB(); }
        }
        if constexpr (j == W0 - 1) { BARRIER(); EPOCH_GET(); }
        if constexpr (j == W0) {
#pragma unroll
          for (int u = 0; u < 4; ++u) *(LAS u32x4*)(L + L_WO + (sw * 32 + u * 8 + (l >> 3)) * ROWP + (l & 7) * 16) = wor[u];
          SB();
        }
        self(self, std::integral_constant<int, j + 1>{});
      }
    };
    phase_a(phase_a, std::integral_constant<int, 0>{});
    STAMP(1);
    asm volatile("" ::"v"(pf));
    for (unsigned spins = 0; *(volatile LAS unsigned*)(L + L_FLAG) == 0u && spins < (SPIN_CAP << 4); ++spins) __builtin_amdgcn_s_sleep(1);
    CFENCE();
    STAMP(4);
    {
      const int dt = (int)((unsigned)__builtin_amdgcn_readfirstlane(*(LAS unsigned*)(L + L_TREF)) - t0);
      tp0 = t0 + (unsigned)(min(max(dt, -400), 400) + P_PACE1 + ((sw * P_PACE) >> 5));
    }
    const u32x4 q0 = *(LAS u32x4*)(L + L_Q + c * 16), q1 = *(LAS u32x4*)(L + L_Q + 256 + c * 16);
    const bool up8 = (c & 8) != 0, up4 = (c & 4) != 0, up2 = (c & 2) != 0, up1 = (c & 1) != 0;
    float u0[4], u1[4], sc[2][2];
    const int hh = c >> 3, jj = c & 7;
    const unsigned pick = (unsigned)((2 * hh + 1) << 8 | (2 * hh));
    const unsigned act = (jj & 1) ? (pick << 16 | 0x0C0Cu) : (0x0C0C0000u | pick);
    u32x4 sel;
    sel.x = (jj >> 1) == 0 ? act : 0x0C0C0C0Cu; sel.y = (jj >> 1) == 1 ? act : 0x0C0C0C0Cu;
    sel.z = (jj >> 1) == 2 ? act : 0x0C0C0C0Cu; sel.w = (jj >> 1) == 3 ? act : 0x0C0C0C0Cu;
    unsigned P0 = 0, P1 = 0;
    f32x4 acc = {0.f, 0.f, 0.f, 0.f};
    u32x4 afr = {0u, 0u, 0u, 0u};
    constexpr float C2 = 0.088388347648318440f * 1.4426950408889634f;
#define COMB(UP, CTRL, LO, HI) ((UP ? HI : LO) + dpp<CTRL>(UP ? LO : HI))
#define AFRAG(I) do { const unsigned t_ = dppu<BCAST + bitrev4((I) & 15)>((I) < 16 ? P0 : P1);                                   \
      afr.x = __builtin_amdgcn_perm(t_, t_, sel.x); afr.y = __builtin_amdgcn_perm(t_, t_, sel.y);                                \
      afr.z = __builtin_amdgcn_perm(t_, t_, sel.z); afr.w = __builtin_amdgcn_perm(t_, t_, sel.w); } while (0)
    auto process = [&](auto ic) {
      constexpr int i = decltype(ic)::value;
      if constexpr (i < 32) {
        const u32x4 kk = R[i];
        float a = dot2(kk.x, q0.x, 0.f), bb = dot2(kk.x, q1.x, 0.f);
        a = dot2(kk.y, q0.y, a); bb = dot2(kk.y, q1.y, bb);
        a = dot2(kk.z, q0.z, a); bb = dot2(kk.z, q1.z, bb);
        a = dot2(kk.w, q0.w, a); bb = dot2(kk.w, q1.w, bb);
        if constexpr (!(i & 1)) { u0[0] = a; u1[0] = bb; }
        else {
          a = COMB(up8, XOR8, u0[0], a); bb = COMB(up8, XOR8, u1[0], bb);
          if constexpr (!(i & 2)) { u0[1] = a; u1[1] = bb; }
          else {
            a = COMB(up4, XOR7, u0[1], a); bb = COMB(up4, XOR7, u1[1], bb);
            if constexpr (!(i & 4)) { u0[2] = a; u1[2] = bb; }
            else {
              a = COMB(up2, XOR2, u0[2], a); bb = COMB(up2, XOR2, u1[2], bb);
              if constexpr (!(i & 8)) { u0[3] = a; u1[3] = bb; }
              else {
                a = COMB(up1, XOR1, u0[3], a); bb = COMB(up1, XOR1, u1[3], bb);
                sc[i >> 4][0] = a; sc[i >> 4][1] = bb;
              }
            }
          }
        }
        if constexpr (i == 31) {
          float m[2], lsum[2], p[2][2];
#pragma unroll
          for (int h = 0; h < 2; ++h) {
            const float s0 = sc[0][h] * C2, s1 = sc[1][h] * C2;
            m[h] = wave_max(fmaxf(s0, s1));
            p[0][h] = __builtin_amdgcn_exp2f(s0 - m[h]); p[1][h] = __builtin_amdgcn_exp2f(s1 - m[h]);
            lsum[h] = wave_sum(p[0][h] + p[1][h]);
          }
          P0 = pk_bf16(p[0][0], p[0][1]); P1 = pk_bf16(p[1][0], p[1][1]);
          if (l == 0) {
            *(LAS f32x4*)(L + L_ML + sw * 16) = f32x4{m[0], m[1], lsum[0], lsum[1]};
            CFENCE();
            *(volatile LAS unsigned*)(L + L_MLF + sw * 4) = 1u;
          }
          AFRAG(0);
          STAMP(5);
        }
      } else {
        acc = mfma16(afr, R[i], acc);
        if constexpr (i < 63) AFRAG(i - 31);
      }
      SB();
    };
    auto phase_b = [&](auto self, auto jc, auto dc) {
      constexpr int j = decltype(jc)::value, done = decltype(dc)::value;
      if constexpr (done < 64) {
        constexpr int landed = j < 64 ? j - WIN : 64;
        auto run = [&](auto self2, auto ic) {
          constexpr int i = decltype(ic)::value;
          if constexpr (i < 64 && i < done + CHUNK && pos(i < 64 ? i : 0) < landed) { process(ic); return self2(self2, std::integral_constant<int, i + 1>{}); }
          else return ic;
        };
        auto nd = run(run, std::integral_constant<int, done>{});
        if constexpr (j < 64) ISSUE(j);
        self(self, std::integral_constant<int, (j < 64 ? j + 1 : 64)>{}, nd);
      }
    };
    phase_b(phase_b, std::integral_constant<int, KB>{}, std::integral_constant<int, 0>{});
#undef COMB
#undef AFRAG
#undef ISSUE
    *(LAS f32x4*)(L + L_RED + sw * 1024 + (g >> 1) * 512 + c * 32 + (g & 1) * 16) = acc;
    STAMP(6);
    BARRIER();
    if (oproj) {
      const int off = c * 1024 + sw * 128 + g * 16;
      __builtin_amdgcn_s_sleep(P_D1);
      SB();
      u32x4 a0 = ld_unit(rws, off, WS_OBOX + kvh * 16384), a1 = ld_unit(rws, off + 64, WS_OBOX + kvh * 16384);
      SB();
      u32x4 wfr[4];
#pragma unroll
      for (int t = 0; t < 4; ++t) wfr[t] = tr_frag(L + L_WO + (sw * 32 + 8 * g + (c >> 2)) * ROWP + t * 32 + (c & 3) * 8, 4 * ROWP);
      SB();
      {
        bool ok = (bad_unit(a0, ta, tb) | bad_unit(a1, ta, tb)) == 0u;
        for (unsigned spins = 0; !__all(ok) && spins < SPIN_CAP; ++spins) {
          CFENCE();
          if (!ok) {
            a0 = ld_unit(rws, off, WS_OBOX + kvh * 16384);
            a1 = ld_unit(rws, off + 64, WS_OBOX + kvh * 16384);
            ok = (bad_unit(a0, ta, tb) | bad_unit(a1, ta, tb)) == 0u;
          }
        }
      }
      STAMP(8);
      const u32x4 of = {a0.x, a0.z, a1.x, a1.z};
#pragma unroll
      for (int t = 0; t < 4; ++t) {
        const f32x4 d = mfma16(wfr[t], of, f32x4{0.f, 0.f, 0.f, 0.f});
        *(LAS f32x4*)(L + L_RED2 + (sw * 4 + t) * 1024 + l16) = d;
      }
      BARRIER();
      if (sw < 4) {
        f32x4 s = *(LAS f32x4*)(L + L_RED2 + sw * 1024 + l16);
#pragma unroll
        for (int ww = 1; ww < 8; ++ww) s += *(LAS f32x4*)(L + L_RED2 + (ww * 4 + sw) * 1024 + l16);
        const bool lo = c < 8;
        const unsigned v0 = __float_as_uint(s.x), v1 = __float_as_uint(s.y), n0 = dppu<XOR8>(__float_as_uint(s.z)), n1 = dppu<XOR8>(__float_as_uint(s.w));
        const int base = WS_PBOX + (((c & 7) * 16 + kvh) * 288 + b * 32 + sw * 8 + g * 2) * 16;
        st_unit(rws, base + (lo ? 0 : 16), u32x4{lo ? v0 : n0, ta, lo ? v1 : n1, tb});
        st_unit(rws, base + 8 * 16 * 288 * 16 + (lo ? 16 : 0), u32x4{lo ? n0 : v0, ta, lo ? n1 : v1, tb});
        STAMP(9);
      } else {
        const int poff = ((kvh * 16 + (l >> 3)) * 288 + b * 32 + (sw - 4) * 8 + (l & 7)) * 16;
        __builtin_amdgcn_s_sleep(P_D2);
        u32x4 p0 = ld_unit(rws, poff, WS_PBOX), p1 = ld_unit(rws, poff + 8 * 288 * 16, WS_PBOX);
        bool ok0 = bad_unit(p0, ta, tb) == 0u, ok1 = bad_unit(p1, ta, tb) == 0u;
        for (unsigned spins = 0; !__all(ok0 && ok1) && spins < SPIN_CAP; ++spins) {
          CFENCE();
          if (!ok0) p0 = ld_unit(rws, poff, WS_PBOX);
          if (!ok1) p1 = ld_unit(rws, poff + 8 * 288 * 16, WS_PBOX);
          ok0 = bad_unit(p0, ta, tb) == 0u; ok1 = bad_unit(p1, ta, tb) == 0u;
        }
        float v0 = __uint_as_float(p0.x) + __uint_as_float(p1.x), v1 = __uint_as_float(p0.z) + __uint_as_float(p1.z);
        v0 += dpp<XOR8>(v0); v1 += dpp<XOR8>(v1);
        v0 = xrow_sum(v0); v1 = xrow_sum(v1);
        if (l < 8) *(unsigned*)(out + kvh * D + b * 64 + (sw - 4) * 16 + l * 2) = pk_bf16(v0, v1);
        if (bid == 0 && tid == 5 * 64) __builtin_amdgcn_raw_buffer_store_b64(u32x2{ta, (unsigned)(e1 >> 32)}, rws, 0, WS_EPOCH, AUX_SC1);
        STAMP(10);
      }
    }
  }
  asm volatile(".Lfused_code_end:");
}

extern "C" void kernel_launch(void* const* d_in, const int* in_sizes, int n_in, void* d_out, int out_size, void* d_ws,
                              size_t ws_size, hipStream_t stream) {
  fused_kernel<<<256, NWAVES * 64, 0, stream>>>((const unsigned short*)d_in[0], (const unsigned short*)d_in[1], (const unsigned short*)d_in[2],
                                                (const unsigned short*)d_in[3], (const unsigned short*)d_in[4], (unsigned short*)d_out,
                                                (unsigned char*)d_ws);
}
```

```cpp
#include <hip/hip_runtime.h>
#include <hip/hip_bf16.h>
#include <cstdint>
#include <type_traits>

constexpr int P_PACE = 118;
constexpr int P_PACE0 = 185;
constexpr int P_PACE1 = 170;
constexpr int P_TPOLL = 660;
constexpr int P_D1 = 14;
constexpr int P_D2 = 25;

constexpr int due(int j) { return (j * P_PACE) >> 2; }

constexpr int B = 16, D = 576, HQ = 32, HKV = 16, SK = 4096, DH = 128, W = 1024;
constexpr int NQ = HQ * DH;
constexpr int NWAVES = 9;
constexpr int W0 = 4;
constexpr int WIN = 6;
constexpr int KB = 30;
constexpr int CHUNK = 3;

typedef unsigned u32x4 __attribute__((ext_vector_type(4)));
typedef unsigned u32x2 __attribute__((ext_vector_type(2)));
typedef float f32x4 __attribute__((ext_vector_type(4)));
typedef short bf16x8 __attribute__((ext_vector_type(8)));
typedef short s16x4 __attribute__((ext_vector_type(4)));
typedef __bf16 bf16x2 __attribute__((ext_vector_type(2)));
#define LAS __attribute__((address_space(3)))

constexpr int AUX_SC1 = 16, AUX_NT = 2;
constexpr int RSRC_W3 = 0x00020000;

constexpr int WS_EPOCH = 0;
constexpr int WS_TBOX = 1024;
constexpr int WS_QBOX = 4096;
constexpr int WS_OBOX = WS_QBOX + 16 * 16 * 1024;
constexpr int WS_PBOX = WS_OBOX + 16 * 16 * 1024;
constexpr int WS_END = WS_PBOX + 16 * 16 * 288 * 16;
constexpr unsigned SPIN_CAP = 1u << 18;
constexpr int bitrev4(int i) { return ((i & 1) << 3) | ((i & 2) << 1) | ((i & 4) >> 1) | ((i & 8) >> 3); }

constexpr int ROWP = 144;
constexpr int L_RED = 0;
constexpr int L_ML = 8192;
constexpr int L_Q = L_ML + 128;
constexpr int L_FLAG = L_Q + 512;
constexpr int L_TREF = L_FLAG + 4;
constexpr int L_MLF = L_FLAG + 16;
constexpr int L_RED2 = 9216;
constexpr int L_WQ = L_RED2;
constexpr int L_WO = L_RED2 + 32768;
constexpr int L_BYTES = L_WO + 256 * ROWP;

__device__ __forceinline__ float dot2(unsigned k, unsigned q, float acc) {
  return __builtin_amdgcn_fdot2_f32_bf16(__builtin_bit_cast(bf16x2, k), __builtin_bit_cast(bf16x2, q), acc, false);
}
__device__ __forceinline__ unsigned pk_bf16(float lo, float hi) {
  bf16x2 v; v.x = (__bf16)lo; v.y = (__bf16)hi;
  return __builtin_bit_cast(unsigned, v);
}
template <int CTRL> __device__ __forceinline__ float dpp(float x) {
  return __builtin_bit_cast(float, __builtin_amdgcn_mov_dpp(__builtin_bit_cast(int, x), CTRL, 0xf, 0xf, true));
}
template <int CTRL> __device__ __forceinline__ unsigned dppu(unsigned x) {
  return (unsigned)__builtin_amdgcn_mov_dpp((int)x, CTRL, 0xf, 0xf, true);
}
constexpr int XOR1 = 0xB1, XOR2 = 0x4E, XOR7 = 0x141, XOR8 = 0x128, BCAST = 0x150;
__device__ __forceinline__ float xrow_max(float x) {
  auto s = __builtin_amdgcn_permlane16_swap(__float_as_uint(x), __float_as_uint(x), false, false);
  x = fmaxf(__uint_as_float(s[0]), __uint_as_float(s[1]));
  auto t = __builtin_amdgcn_permlane32_swap(__float_as_uint(x), __float_as_uint(x), false, false);
  return fmaxf(__uint_as_float(t[0]), __uint_as_float(t[1]));
}
__device__ __forceinline__ float xrow_sum(float x) {
  auto s = __builtin_amdgcn_permlane16_swap(__float_as_uint(x), __float_as_uint(x), false, false);
  x = __uint_as_float(s[0]) + __uint_as_float(s[1]);
  auto t = __builtin_amdgcn_permlane32_swap(__float_as_uint(x), __float_as_uint(x), false, false);
  return __uint_as_float(t[0]) + __uint_as_float(t[1]);
}
__device__ __forceinline__ float wave_max(float x) {
  x = fmaxf(x, dpp<XOR1>(x)); x = fmaxf(x, dpp<XOR2>(x)); x = fmaxf(x, dpp<XOR7>(x)); x = fmaxf(x, dpp<XOR8>(x));
  return xrow_max(x);
}
__device__ __forceinline__ float wave_sum(float x) {
  x += dpp<XOR1>(x); x += dpp<XOR2>(x); x += dpp<XOR7>(x); x += dpp<XOR8>(x);
  return xrow_sum(x);
}
__device__ __forceinline__ f32x4 mfma16(u32x4 a, u32x4 b, f32x4 c) {
  return __builtin_amdgcn_mfma_f32_16x16x32_bf16(__builtin_bit_cast(bf16x8, a), __builtin_bit_cast(bf16x8, b), c, 0, 0, 0);
}
__device__ __forceinline__ __amdgpu_buffer_rsrc_t make_rsrc(const void* p, int bytes) {
  return __builtin_amdgcn_make_buffer_rsrc((void*)p, 0, bytes, RSRC_W3);
}
__device__ __forceinline__ u32x4 tr_frag(LAS unsigned char* p, int second) {
  s16x4 lo = __builtin_amdgcn_ds_read_tr16_b64_v4i16((LAS s16x4*)p);
  s16x4 hi = __builtin_amdgcn_ds_read_tr16_b64_v4i16((LAS s16x4*)(p + second));
  u32x2 a = __builtin_bit_cast(u32x2, lo), b = __builtin_bit_cast(u32x2, hi);
  return u32x4{a.x, a.y, b.x, b.y};
}
__device__ __forceinline__ unsigned bad_unit(u32x4 v, unsigned ta, unsigned tb) { return (v.y ^ ta) | (v.w ^ tb); }
__device__ __forceinline__ void st_unit(__amdgpu_buffer_rsrc_t r, int off, u32x4 v) { __builtin_amdgcn_raw_buffer_store_b128(v, r, off, 0, AUX_SC1); }
__device__ __forceinline__ u32x4 ld_unit(__amdgpu_buffer_rsrc_t r, int off, int soff) { return __builtin_amdgcn_raw_buffer_load_b128(r, off, soff, AUX_SC1); }
#define SB() __builtin_amdgcn_sched_barrier(0)
#define CFENCE() asm volatile("" ::: "memory")
#define BARRIER() do { CFENCE(); asm volatile("s_waitcnt lgkmcnt(0)" ::: "memory"); __builtin_amdgcn_s_barrier(); CFENCE(); SB(); } while (0)
template <int N> __device__ __forceinline__ void wait_vm() { asm volatile("s_waitcnt vmcnt(%0)" ::"n"(N) : "memory"); }
__device__ __forceinline__ void pace_until(unsigned due) {
  asm volatile("1:\n\ts_memrealtime s[96:97]\n\ts_waitcnt lgkmcnt(0)\n\ts_sub_u32 s96, s96, %0\n\ts_cmp_lt_i32 s96, 0\n\ts_cbranch_scc1 1b"
               :: "s"(due) : "s96", "s97", "scc", "memory");
}

__global__ void __launch_bounds__(NWAVES * 64, 3)
fused_kernel(const unsigned short* __restrict__ x, const unsigned short* __restrict__ kc, const unsigned short* __restrict__ vc,
             const unsigned short* __restrict__ wq, const unsigned short* __restrict__ wo, unsigned short* __restrict__ out,
             unsigned char* __restrict__ ws) {
  __shared__ __attribute__((aligned(16))) unsigned char smem[L_BYTES];
  LAS unsigned char* L = (LAS unsigned char*)smem;
  const int bid = blockIdx.x;
  const int kvh = (bid & 7) * 2 + ((bid >> 3) & 1), b = bid >> 4;
  const int tid = threadIdx.x, l = tid & 63, w = __builtin_amdgcn_readfirstlane(tid >> 6), g = l >> 4, c = l & 15;
  const int sw = w - 1;
  unsigned pf;
  {
    unsigned long long pc; unsigned clen;
    asm volatile("s_getpc_b64 %0\n.Lfused_code_begin:\n\ts_mov_b32 %1, .Lfused_code_end-.Lfused_code_begin" : "=s"(pc), "=s"(clen));
    pf = __builtin_amdgcn_raw_buffer_load_b32(make_rsrc((const void*)pc, (int)clen), tid < 8 ? (tid * 32 + (bid >> 3)) * 128 : 0x7fff0000, 0, 0);
  }
  unsigned long long t64 = __builtin_amdgcn_s_memrealtime();
  *(LAS unsigned*)(L + L_FLAG + (l & 15) * 4) = 0u;
  const bool oproj = b < 9;
  const int offa_s = ((b * HKV + kvh) * SK + (SK - W) - 4) * (DH * 2) + tid * 16, offa_q = (l >> 1) * (NQ * 2) + (l & 1) * 16 + (kvh * 256 + b * 16) * 2;
  const int offb_s = (kvh * 256 - 32 + (tid >> 6) * 32 + (l >> 3)) * (D * 2) + (oproj ? b * 128 : 0x7f000000) + (l & 7) * 16, offb_q = c * (D * 2) + g * 16;
  int offa = tid < 64 ? offa_q : offa_s, offb = tid < 64 ? offb_q : offb_s;
  const int rot8k = (bid >> 3) * 8192;
  asm volatile("" : "+v"(offa), "+v"(offb));
  SB();
  asm volatile("" : "+s"(t64) : "s"(x), "s"(kc), "s"(vc), "s"(wq), "s"(wo), "s"(out), "s"(ws));
  const unsigned t0 = (unsigned)t64;
  const __amdgpu_buffer_rsrc_t rws = make_rsrc(ws, WS_END);
  const int l16 = l * 16;
  unsigned long long e1;
  unsigned ta, tb;
#define EPOCH_GET() do { asm volatile("s_load_dwordx2 %0, %1, 0x0\n\ts_waitcnt lgkmcnt(0)" : "=s"(e1) : "s"(ws + WS_EPOCH) : "memory"); SB(); \
    e1 += 1ull; ta = (unsigned)e1; tb = (unsigned)(e1 >> 32) ^ (ta * 0x9E3779B1u); } while (0)

  if (w == 0) {
    __builtin_amdgcn_s_setprio(3);
    {
      const __amdgpu_buffer_rsrc_t rwq = make_rsrc(wq, D * NQ * 2);
      const __amdgpu_buffer_rsrc_t rx = make_rsrc(x, B * D * 2);
      u32x4 wr[18], xr[18];
#pragma unroll
      for (int s = 0; s < 18; ++s) {
        wr[s] = __builtin_amdgcn_raw_buffer_load_b128(rwq, offa, s * 32 * NQ * 2, AUX_NT); SB();
        xr[s] = __builtin_amdgcn_raw_buffer_load_b128(rx, offb + s * 64, 0, 0); SB();
        if (s == 2) BARRIER();
      }
      EPOCH_GET();
      if (bid < 8 && l < 8) st_unit(rws, WS_TBOX + bid * 128 + l16, u32x4{t0, ta, t0, tb});
#pragma unroll
      for (int s = 0; s < 18; ++s) *(LAS u32x4*)(L + L_WQ + s * 1024 + l16) = wr[s];
      f32x4 acc = {0.f, 0.f, 0.f, 0.f};
      const int troff = (8 * g + (c >> 2)) * 32 + (c & 3) * 8;
#pragma unroll
      for (int s = 0; s < 18; ++s) acc = mfma16(tr_frag(L + L_WQ + s * 1024 + troff, 128), xr[s], acc);
      st_unit(rws, WS_QBOX + kvh * 16384 + c * 1024 + (b * 4 + g) * 16, u32x4{pk_bf16(acc[0], acc[1]), ta, pk_bf16(acc[2], acc[3]), tb});
    }
    asm volatile("" ::"v"(pf));
    while ((unsigned)__builtin_amdgcn_s_memrealtime() - t0 < (unsigned)P_TPOLL) __builtin_amdgcn_s_sleep(4);
    {
      const u32x4 tu = ld_unit(rws, (l & 7) * 128, WS_TBOX);
      u32x4 qa = ld_unit(rws, l16, WS_QBOX + (kvh * 16 + b) * 1024);
      int d = bad_unit(tu, ta, tb) == 0u ? (int)(tu.x - t0) : 0;
      d = min(max(d, -300), 300);
      d += __builtin_amdgcn_mov_dpp(d, XOR1, 0xf, 0xf, true); d += __builtin_amdgcn_mov_dpp(d, XOR2, 0xf, 0xf, true);
      d += __builtin_amdgcn_mov_dpp(d, XOR7, 0xf, 0xf, true); d >>= 3;
      *(LAS unsigned*)(L + L_TREF) = t0 + (unsigned)d;
      for (unsigned spins = 0; !__all(bad_unit(qa, ta, tb) == 0u) && spins < SPIN_CAP; ++spins) {
        __builtin_amdgcn_s_sleep(8);
        CFENCE();
        qa = ld_unit(rws, l16, WS_QBOX + (kvh * 16 + b) * 1024);
      }
      *(LAS u32x2*)(L + L_Q + l * 8) = u32x2{qa.x, qa.z};
      asm volatile("s_waitcnt lgkmcnt(0)" ::: "memory");
      *(volatile LAS unsigned*)(L + L_FLAG) = 1u;
    }
    __builtin_amdgcn_s_setprio(0);
    for (unsigned spins = 0; !__all(*(volatile LAS unsigned*)(L + L_MLF + (l & 7) * 4) != 0u) && spins < (SPIN_CAP << 4); ++spins) __builtin_amdgcn_s_sleep(16);
    CFENCE();
    float fw[8];
    {
      const int h = l >> 5;
      float mw[8], lw[8];
#pragma unroll
      for (int ww = 0; ww < 8; ++ww) { mw[ww] = *(LAS float*)(L + L_ML + ww * 16 + h * 4); lw[ww] = *(LAS float*)(L + L_ML + ww * 16 + 8 + h * 4); }
      float M = mw[0];
#pragma unroll
      for (int ww = 1; ww < 8; ++ww) M = fmaxf(M, mw[ww]);
      float Ls = 0.f;
#pragma unroll
      for (int ww = 0; ww < 8; ++ww) { fw[ww] = __builtin_amdgcn_exp2f(mw[ww] - M); Ls = fmaf(fw[ww], lw[ww], Ls); }
      const float inv = 1.0f / Ls;
#pragma unroll
      for (int ww = 0; ww < 8; ++ww) { fw[ww] *= inv; asm volatile("" : "+v"(fw[ww])); }
    }
    int ooff = WS_OBOX + (kvh * 16 + b) * 1024 + (l >> 3) * 128 + (l & 1) * 64 + ((l >> 1) & 3) * 16;
    asm volatile("" : "+v"(ooff));
    __builtin_amdgcn_s_setprio(3);
    BARRIER();
    f32x4 ow[8];
#pragma unroll
    for (int ww = 0; ww < 8; ++ww) ow[ww] = *(LAS f32x4*)(L + L_RED + ww * 1024 + l16);
    const f32x4 O = ((fw[0] * ow[0] + fw[1] * ow[1]) + (fw[2] * ow[2] + fw[3] * ow[3])) + ((fw[4] * ow[4] + fw[5] * ow[5]) + (fw[6] * ow[6] + fw[7] * ow[7]));
    st_unit(rws, ooff, u32x4{pk_bf16(O.x, O.y), ta, pk_bf16(O.z, O.w), tb});
    if (oproj) BARRIER();
  } else {
    const __amdgpu_buffer_rsrc_t rk = make_rsrc(kc, B * HKV * SK * DH * 2);
    const __amdgpu_buffer_rsrc_t rv = make_rsrc(vc, B * HKV * SK * DH * 2);
    unsigned tp0 = t0 + (unsigned)(P_PACE0 + ((sw * P_PACE) >> 5));
    u32x4 R[64];
    const __amdgpu_buffer_rsrc_t rwo = make_rsrc(wo, NQ * D * 2);
    u32x4 wor[4];
    constexpr auto item = [](int j) constexpr { return j == 0 ? 0 : (j == 1 ? 32 : (j <= 32 ? j - 1 : j)); };
    constexpr auto pos = [](int i) constexpr { return i == 0 ? 0 : (i == 32 ? 1 : (i < 32 ? i + 1 : i)); };
#define ISSUE(j) do { constexpr int it_ = item(j);                                                                               \
      if constexpr ((j) >= W0) { wait_vm<WIN - 1>(); pace_until(tp0 + (unsigned)due(j)); SB(); }                                 \
      R[it_] = __builtin_amdgcn_raw_buffer_load_b128(it_ < 32 ? rk : rv, offa, (it_ * 8192 + rot8k) & 0x3ffff, AUX_NT); SB(); } while (0)
    auto phase_a = [&](auto self, auto jc) {
      constexpr int j = decltype(jc)::value;
      if constexpr (j < KB) {
        ISSUE(j);
        if constexpr (j == 1) {
#pragma unroll
          for (int u = 0; u < 4; ++u) { wor[u] = __builtin_amdgcn_raw_buffer_load_b128(rwo, offb, u * 8 * D * 2, AUX_NT); SB(); }
        }
        if constexpr (j == W0 - 1) { BARRIER(); EPOCH_GET(); }
        if constexpr (j == W0) {
#pragma unroll
          for (int u = 0; u < 4; ++u) *(LAS u32x4*)(L + L_WO + (sw * 32 + u * 8 + (l >> 3)) * ROWP + (l & 7) * 16) = wor[u];
          SB();
        }
        self(self, std::integral_constant<int, j + 1>{});
      }
    };
    phase_a(phase_a, std::integral_constant<int, 0>{});
    asm volatile("" ::"v"(pf));
    for (unsigned spins = 0; *(volatile LAS unsigned*)(L + L_FLAG) == 0u && spins < (SPIN_CAP << 4); ++spins) __builtin_amdgcn_s_sleep(1);
    CFENCE();
    {
      const int dt = (int)((unsigned)__builtin_amdgcn_readfirstlane(*(LAS unsigned*)(L + L_TREF)) - t0);
      tp0 = t0 + (unsigned)(min(max(dt, -400), 400) + P_PACE1 + ((sw * P_PACE) >> 5));
    }
    const u32x4 q0 = *(LAS u32x4*)(L + L_Q + c * 16), q1 = *(LAS u32x4*)(L + L_Q + 256 + c * 16);
    const bool up8 = (c & 8) != 0, up4 = (c & 4) != 0, up2 = (c & 2) != 0, up1 = (c & 1) != 0;
    float u0[4], u1[4], sc[2][2];
    const int hh = c >> 3, jj = c & 7;
    const unsigned pick = (unsigned)((2 * hh + 1) << 8 | (2 * hh));
    const unsigned act = (jj & 1) ? (pick << 16 | 0x0C0Cu) : (0x0C0C0000u | pick);
    u32x4 sel;
    sel.x = (jj >> 1) == 0 ? act : 0x0C0C0C0Cu; sel.y = (jj >> 1) == 1 ? act : 0x0C0C0C0Cu;
    sel.z = (jj >> 1) == 2 ? act : 0x0C0C0C0Cu; sel.w = (jj >> 1) == 3 ? act : 0x0C0C0C0Cu;
    unsigned P0 = 0, P1 = 0;
    f32x4 acc = {0.f, 0.f, 0.f, 0.f};
    u32x4 afr = {0u, 0u, 0u, 0u};
    constexpr float C2 = 0.088388347648318440f * 1.4426950408889634f;
#define COMB(UP, CTRL, LO, HI) ((UP ? HI : LO) + dpp<CTRL>(UP ? LO : HI))
#define AFRAG(I) do { const unsigned t_ = dppu<BCAST + bitrev4((I) & 15)>((I) < 16 ? P0 : P1);                                   \
      afr.x = __builtin_amdgcn_perm(t_, t_, sel.x); afr.y = __builtin_amdgcn_perm(t_, t_, sel.y);                                \
      afr.z = __builtin_amdgcn_perm(t_, t_, sel.z); afr.w = __builtin_amdgcn_perm(t_, t_, sel.w); } while (0)
    auto process = [&](auto ic) {
      constexpr int i = decltype(ic)::value;
      if constexpr (i < 32) {
        const u32x4 kk = R[i];
        float a = dot2(kk.x, q0.x, 0.f), bb = dot2(kk.x, q1.x, 0.f);
        a = dot2(kk.y, q0.y, a); bb = dot2(kk.y, q1.y, bb);
        a = dot2(kk.z, q0.z, a); bb = dot2(kk.z, q1.z, bb);
        a = dot2(kk.w, q0.w, a); bb = dot2(kk.w, q1.w, bb);
        if constexpr (!(i & 1)) { u0[0] = a; u1[0] = bb; }
        else {
          a = COMB(up8, XOR8, u0[0], a); bb = COMB(up8, XOR8, u1[0], bb);
          if constexpr (!(i & 2)) { u0[1] = a; u1[1] = bb; }
          else {
            a = COMB(up4, XOR7, u0[1], a); bb = COMB(up4, XOR7, u1[1], bb);
            if constexpr (!(i & 4)) { u0[2] = a; u1[2] = bb; }
            else {
              a = COMB(up2, XOR2, u0[2], a); bb = COMB(up2, XOR2, u1[2], bb);
              if constexpr (!(i & 8)) { u0[3] = a; u1[3] = bb; }
              else {
                a = COMB(up1, XOR1, u0[3], a); bb = COMB(up1, XOR1, u1[3], bb);
                sc[i >> 4][0] = a; sc[i >> 4][1] = bb;
              }
            }
          }
        }
        if constexpr (i == 31) {
          float m[2], lsum[2], p[2][2];
#pragma unroll
          for (int h = 0; h < 2; ++h) {
            const float s0 = sc[0][h] * C2, s1 = sc[1][h] * C2;
            m[h] = wave_max(fmaxf(s0, s1));
            p[0][h] = __builtin_amdgcn_exp2f(s0 - m[h]); p[1][h] = __builtin_amdgcn_exp2f(s1 - m[h]);
            lsum[h] = wave_sum(p[0][h] + p[1][h]);
          }
          P0 = pk_bf16(p[0][0], p[0][1]); P1 = pk_bf16(p[1][0], p[1][1]);
          if (l == 0) {
            *(LAS f32x4*)(L + L_ML + sw * 16) = f32x4{m[0], m[1], lsum[0], lsum[1]};
            CFENCE();
            *(volatile LAS unsigned*)(L + L_MLF + sw * 4) = 1u;
          }
          AFRAG(0);
        }
      } else {
        acc = mfma16(afr, R[i], acc);
        if constexpr (i < 63) AFRAG(i - 31);
      }
      SB();
    };
    auto phase_b = [&](auto self, auto jc, auto dc) {
      constexpr int j = decltype(jc)::value, done = decltype(dc)::value;
      if constexpr (done < 64) {
        constexpr int landed = j < 64 ? j - WIN : 64;
        auto run = [&](auto self2, auto ic) {
          constexpr int i = decltype(ic)::value;
          if constexpr (i < 64 && i < done + CHUNK && pos(i < 64 ? i : 0) < landed) { process(ic); return self2(self2, std::integral_constant<int, i + 1>{}); }
          else return ic;
        };
        auto nd = run(run, std::integral_constant<int, done>{});
        if constexpr (j < 64) ISSUE(j);
        self(self, std::integral_constant<int, (j < 64 ? j + 1 : 64)>{}, nd);
      }
    };
    phase_b(phase_b, std::integral_constant<int, KB>{}, std::integral_constant<int, 0>{});
#undef COMB
#undef AFRAG
#undef ISSUE
    *(LAS f32x4*)(L + L_RED + sw * 1024 + (g >> 1) * 512 + c * 32 + (g & 1) * 16) = acc;
    BARRIER();
    if (oproj) {
      const int off = c * 1024 + sw * 128 + g * 16;
      __builtin_amdgcn_s_sleep(P_D1);
      SB();
      u32x4 a0 = ld_unit(rws, off, WS_OBOX + kvh * 16384), a1 = ld_unit(rws, off + 64, WS_OBOX + kvh * 16384);
      SB();
      u32x4 wfr[4];
#pragma unroll
      for (int t = 0; t < 4; ++t) wfr[t] = tr_frag(L + L_WO + (sw * 32 + 8 * g + (c >> 2)) * ROWP + t * 32 + (c & 3) * 8, 4 * ROWP);
      SB();
      {
        bool ok = (bad_unit(a0, ta, tb) | bad_unit(a1, ta, tb)) == 0u;
        for (unsigned spins = 0; !__all(ok) && spins < SPIN_CAP; ++spins) {
          CFENCE();
          if (!ok) {
            a0 = ld_unit(rws, off, WS_OBOX + kvh * 16384);
            a1 = ld_unit(rws, off + 64, WS_OBOX + kvh * 16384);
            ok = (bad_unit(a0, ta, tb) | bad_unit(a1, ta, tb)) == 0u;
          }
        }
      }
      const u32x4 of = {a0.x, a0.z, a1.x, a1.z};
#pragma unroll
      for (int t = 0; t < 4; ++t) {
        const f32x4 d = mfma16(wfr[t], of, f32x4{0.f, 0.f, 0.f, 0.f});
        *(LAS f32x4*)(L + L_RED2 + (sw * 4 + t) * 1024 + l16) = d;
      }
      BARRIER();
      if (sw < 4) {
        f32x4 s = *(LAS f32x4*)(L + L_RED2 + sw * 1024 + l16);
#pragma unroll
        for (int ww = 1; ww < 8; ++ww) s += *(LAS f32x4*)(L + L_RED2 + (ww * 4 + sw) * 1024 + l16);
        const bool lo = c < 8;
        const unsigned v0 = __float_as_uint(s.x), v1 = __float_as_uint(s.y), n0 = dppu<XOR8>(__float_as_uint(s.z)), n1 = dppu<XOR8>(__float_as_uint(s.w));
        const int base = WS_PBOX + (((c & 7) * 16 + kvh) * 288 + b * 32 + sw * 8 + g * 2) * 16;
        st_unit(rws, base + (lo ? 0 : 16), u32x4{lo ? v0 : n0, ta, lo ? v1 : n1, tb});
        st_unit(rws, base + 8 * 16 * 288 * 16 + (lo ? 16 : 0), u32x4{lo ? n0 : v0, ta, lo ? n1 : v1, tb});
      } else {
        const int poff = ((kvh * 16 + (l >> 3)) * 288 + b * 32 + (sw - 4) * 8 + (l & 7)) * 16;
        __builtin_amdgcn_s_sleep(P_D2);
        u32x4 p0 = ld_unit(rws, poff, WS_PBOX), p1 = ld_unit(rws, poff + 8 * 288 * 16, WS_PBOX);
        bool ok0 = bad_unit(p0, ta, tb) == 0u, ok1 = bad_unit(p1, ta, tb) == 0u;
        for (unsigned spins = 0; !__all(ok0 && ok1) && spins < SPIN_CAP; ++spins) {
          CFENCE();
          if (!ok0) p0 = ld_unit(rws, poff, WS_PBOX);
          if (!ok1) p1 = ld_unit(rws, poff + 8 * 288 * 16, WS_PBOX);
          ok0 = bad_unit(p0, ta, tb) == 0u; ok1 = bad_unit(p1, ta, tb) == 0u;
        }
        float v0 = __uint_as_float(p0.x) + __uint_as_float(p1.x), v1 = __uint_as_float(p0.z) + __uint_as_float(p1.z);
        v0 += dpp<XOR8>(v0); v1 += dpp<XOR8>(v1);
        v0 = xrow_sum(v0); v1 = xrow_sum(v1);
        if (l < 8) *(unsigned*)(out + kvh * D + b * 64 + (sw - 4) * 16 + l * 2) = pk_bf16(v0, v1);
        if (bid == 0 && tid == 5 * 64) __builtin_amdgcn_raw_buffer_store_b64(u32x2{ta, (unsigned)(e1 >> 32)}, rws, 0, WS_EPOCH, AUX_SC1);
      }
    }
  }
  asm volatile(".Lfused_code_end:");
}

extern "C" void kernel_launch(void* const* d_in, const int* in_sizes, int n_in, void* d_out, int out_size, void* d_ws,
                              size_t ws_size, hipStream_t stream) {
  fused_kernel<<<256, NWAVES * 64, 0, stream>>>((const unsigned short*)d_in[0], (const unsigned short*)d_in[1], (const unsigned short*)d_in[2],
                                                (const unsigned short*)d_in[3], (const unsigned short*)d_in[4], (unsigned short*)d_out,
                                                (unsigned char*)d_ws);
}
```

```cpp
#include <hip/hip_runtime.h>
#include <hip/hip_bf16.h>
#include <cstdint>
#include <type_traits>

constexpr int P_PACE = 118;
constexpr int P_PACE0 = 185;
constexpr int P_PACE1 = 170;
constexpr int P_TPOLL = 660;
constexpr int P_D1 = 14;
constexpr int P_D2 = 25;

constexpr int due(int j) { return (j * P_PACE) >> 2; }

constexpr int B = 16, D = 576, HQ = 32, HKV = 16, SK = 4096, DH = 128, W = 1024;
constexpr int NQ = HQ * DH;
constexpr int NWAVES = 9;
constexpr int W0 = 4;
constexpr int WIN = 6;
constexpr int KB = 30;
constexpr int CHUNK = 3;

typedef unsigned u32x4 __attribute__((ext_vector_type(4)));
typedef unsigned u32x2 __attribute__((ext_vector_type(2)));
typedef float f32x4 __attribute__((ext_vector_type(4)));
typedef short bf16x8 __attribute__((ext_vector_type(8)));
typedef short s16x4 __attribute__((ext_vector_type(4)));
typedef __bf16 bf16x2 __attribute__((ext_vector_type(2)));
#define LAS __attribute__((address_space(3)))

constexpr int AUX_SC1 = 16, AUX_NT = 2;
constexpr int RSRC_W3 = 0x00020000;

constexpr int WS_EPOCH = 0;
constexpr int WS_TBOX = 1024;
constexpr int WS_QBOX = 4096;
constexpr int WS_OBOX = WS_QBOX + 16 * 16 * 1024;
constexpr int WS_PBOX = WS_OBOX + 16 * 16 * 1024;
constexpr int WS_END = WS_PBOX + 16 * 16 * 288 * 16;
constexpr unsigned SPIN_CAP = 1u << 18;
constexpr int bitrev4(int i) { return ((i & 1) << 3) | ((i & 2) << 1) | ((i & 4) >> 1) | ((i & 8) >> 3); }

constexpr int ROWP = 144;
constexpr int L_RED = 0;
constexpr int L_ML = 8192;
constexpr int L_Q = L_ML + 128;
constexpr int L_FLAG = L_Q + 512;
constexpr int L_TREF = L_FLAG + 4;
constexpr int L_MLF = L_FLAG + 16;
constexpr int L_RED2 = 9216;
constexpr int L_WQ = L_RED2;
constexpr int L_WO = L_RED2 + 32768;
constexpr int L_BYTES = L_WO + 256 * ROWP;

__device__ __forceinline__ float dot2(unsigned k, unsigned q, float acc) {
  return __builtin_amdgcn_fdot2_f32_bf16(__builtin_bit_cast(bf16x2, k), __builtin_bit_cast(bf16x2, q), acc, false);
}
__device__ __forceinline__ unsigned pk_bf16(float lo, float hi) {
  bf16x2 v; v.x = (__bf16)lo; v.y = (__bf16)hi;
  return __builtin_bit_cast(unsigned, v);
}
template <int CTRL> __device__ __forceinline__ float dpp(float x) {
  return __builtin_bit_cast(float, __builtin_amdgcn_mov_dpp(__builtin_bit_cast(int, x), CTRL, 0xf, 0xf, true));
}
template <int CTRL> __device__ __forceinline__ unsigned dppu(unsigned x) {
  return (unsigned)__builtin_amdgcn_mov_dpp((int)x, CTRL, 0xf, 0xf, true);
}
constexpr int XOR1 = 0xB1, XOR2 = 0x4E, XOR7 = 0x141, XOR8 = 0x128, BCAST = 0x150;
__device__ __forceinline__ float xrow_max(float x) {
  auto s = __builtin_amdgcn_permlane16_swap(__float_as_uint(x), __float_as_uint(x), false, false);
  x = fmaxf(__uint_as_float(s[0]), __uint_as_float(s[1]));
  auto t = __builtin_amdgcn_permlane32_swap(__float_as_uint(x), __float_as_uint(x), false, false);
  return fmaxf(__uint_as_float(t[0]), __uint_as_float(t[1]));
}
__device__ __forceinline__ float xrow_sum(float x) {
  auto s = __builtin_amdgcn_permlane16_swap(__float_as_uint(x), __float_as_uint(x), false, false);
  x = __uint_as_float(s[0]) + __uint_as_float(s[1]);
  auto t = __builtin_amdgcn_permlane32_swap(__float_as_uint(x), __float_as_uint(x), false, false);
  return __uint_as_float(t[0]) + __uint_as_float(t[1]);
}
__device__ __forceinline__ float wave_max(float x) {
  x = fmaxf(x, dpp<XOR1>(x)); x = fmaxf(x, dpp<XOR2>(x)); x = fmaxf(x, dpp<XOR7>(x)); x = fmaxf(x, dpp<XOR8>(x));
  return xrow_max(x);
}
__device__ __forceinline__ float wave_sum(float x) {
  x += dpp<XOR1>(x); x += dpp<XOR2>(x); x += dpp<XOR7>(x); x += dpp<XOR8>(x);
  return xrow_sum(x);
}
__device__ __forceinline__ f32x4 mfma16(u32x4 a, u32x4 b, f32x4 c) {
  return __builtin_amdgcn_mfma_f32_16x16x32_bf16(__builtin_bit_cast(bf16x8, a), __builtin_bit_cast(bf16x8, b), c, 0, 0, 0);
}
__device__ __forceinline__ __amdgpu_buffer_rsrc_t make_rsrc(const void* p, int bytes) {
  return __builtin_amdgcn_make_buffer_rsrc((void*)p, 0, bytes, RSRC_W3);
}
__device__ __forceinline__ u32x4 tr_frag(LAS unsigned char* p, int second) {
  s16x4 lo = __builtin_amdgcn_ds_read_tr16_b64_v4i16((LAS s16x4*)p);
  s16x4 hi = __builtin_amdgcn_ds_read_tr16_b64_v4i16((LAS s16x4*)(p + second));
  u32x2 a = __builtin_bit_cast(u32x2, lo), b = __builtin_bit_cast(u32x2, hi);
  return u32x4{a.x, a.y, b.x, b.y};
}
__device__ __forceinline__ unsigned bad_unit(u32x4 v, unsigned ta, unsigned tb) { return (v.y ^ ta) | (v.w ^ tb); }
__device__ __forceinline__ void st_unit(__amdgpu_buffer_rsrc_t r, int off, u32x4 v) { __builtin_amdgcn_raw_buffer_store_b128(v, r, off, 0, AUX_SC1); }
__device__ __forceinline__ u32x4 ld_unit(__amdgpu_buffer_rsrc_t r, int off, int soff) { return __builtin_amdgcn_raw_buffer_load_b128(r, off, soff, AUX_SC1); }
#define SB() __builtin_amdgcn_sched_barrier(0)
#define CFENCE() asm volatile("" ::: "memory")
#define BARRIER() do { CFENCE(); asm volatile("s_waitcnt lgkmcnt(0)" ::: "memory"); __builtin_amdgcn_s_barrier(); CFENCE(); SB(); } while (0)
template <int N> __device__ __forceinline__ void wait_vm() { asm volatile("s_waitcnt vmcnt(%0)" ::"n"(N) : "memory"); }
__device__ __forceinline__ void pace_until(unsigned due) {
  asm volatile("1:\n\ts_memrealtime s[96:97]\n\ts_waitcnt lgkmcnt(0)\n\ts_sub_u32 s96, s96, %0\n\ts_cmp_lt_i32 s96, 0\n\ts_cbranch_scc1 1b"
               :: "s"(due) : "s96", "s97", "scc", "memory");
}

__global__ void __launch_bounds__(NWAVES * 64, 3)
fused_kernel(const unsigned short* __restrict__ x, const unsigned short* __restrict__ kc, const unsigned short* __restrict__ vc,
             const unsigned short* __restrict__ wq, const unsigned short* __restrict__ wo, unsigned short* __restrict__ out,
             unsigned char* __restrict__ ws) {
  __shared__ __attribute__((aligned(16))) unsigned char smem[L_BYTES];
  LAS unsigned char* L = (LAS unsigned char*)smem;
  const int bid = blockIdx.x;
  const int kvh = (bid & 7) * 2 + ((bid >> 3) & 1), b = bid >> 4;
  const int tid = threadIdx.x, l = tid & 63, w = __builtin_amdgcn_readfirstlane(tid >> 6), g = l >> 4, c = l & 15;
  const int sw = w - 1;
  unsigned pf;
  {
    unsigned long long pc; unsigned clen;
    asm volatile("s_getpc_b64 %0\n.Lfused_code_begin:\n\ts_mov_b32 %1, .Lfused_code_end-.Lfused_code_begin" : "=s"(pc), "=s"(clen));
    pf = __builtin_amdgcn_raw_buffer_load_b32(make_rsrc((const void*)pc, (int)clen), tid < 8 ? (tid * 32 + (bid >> 3)) * 128 : 0x7fff0000, 0, 0);
  }
  unsigned long long t64 = __builtin_amdgcn_s_memrealtime();
  *(LAS unsigned*)(L + L_FLAG + (l & 15) * 4) = 0u;
  const bool oproj = b < 9;
  const int offa_s = ((b * HKV + kvh) * SK + (SK - W) - 4) * (DH * 2) + tid * 16, offa_q = (l >> 1) * (NQ * 2) + (l & 1) * 16 + (kvh * 256 + b * 16) * 2;
  const int offb_s = (kvh * 256 - 32 + (tid >> 6) * 32 + (l >> 3)) * (D * 2) + (oproj ? b * 128 : 0x7f000000) + (l & 7) * 16, offb_q = c * (D * 2) + g * 16;
  int offa = tid < 64 ? offa_q : offa_s, offb = tid < 64 ? offb_q : offb_s;
  const int rot8k = (bid >> 3) * 8192;
  asm volatile("" : "+v"(offa), "+v"(offb));
  SB();
  asm volatile("" : "+s"(t64) : "s"(x), "s"(kc), "s"(vc), "s"(wq), "s"(wo), "s"(out), "s"(ws));
  const unsigned t0 = (unsigned)t64;
  const __amdgpu_buffer_rsrc_t rws = make_rsrc(ws, WS_END);
  const int l16 = l * 16;
  unsigned long long e1;
  unsigned ta, tb;
#define EPOCH_GET() do { asm volatile("s_load_dwordx2 %0, %1, 0x0\n\ts_waitcnt lgkmcnt(0)" : "=s"(e1) : "s"(ws + WS_EPOCH) : "memory"); SB(); \
    e1 += 1ull; ta = (unsigned)e1; tb = (unsigned)(e1 >> 32) ^ (ta * 0x9E3779B1u); } while (0)

  if (w == 0) {
    __builtin_amdgcn_s_setprio(3);
    {
      const __amdgpu_buffer_rsrc_t rwq = make_rsrc(wq, D * NQ * 2);
      const __amdgpu_buffer_rsrc_t rx = make_rsrc(x, B * D * 2);
      u32x4 wr[18], xr[18];
#pragma unroll
      for (int s = 0; s < 18; ++s) {
        wr[s] = __builtin_amdgcn_raw_buffer_load_b128(rwq, offa, s * 32 * NQ * 2, AUX_NT); SB();
        xr[s] = __builtin_amdgcn_raw_buffer_load_b128(rx, offb + s * 64, 0, 0); SB();
        if (s == 2) BARRIER();
      }
      EPOCH_GET();
      if (bid < 8 && l < 8) st_unit(rws, WS_TBOX + bid * 128 + l16, u32x4{t0, ta, t0, tb});
#pragma unroll
      for (int s = 0; s < 18; ++s) *(LAS u32x4*)(L + L_WQ + s * 1024 + l16) = wr[s];
      f32x4 acc = {0.f, 0.f, 0.f, 0.f};
      const int troff = (8 * g + (c >> 2)) * 32 + (c & 3) * 8;
#pragma unroll
      for (int s = 0; s < 18; ++s) acc = mfma16(tr_frag(L + L_WQ + s * 1024 + troff, 128), xr[s], acc);
      st_unit(rws, WS_QBOX + kvh * 16384 + c * 1024 + (b * 4 + g) * 16, u32x4{pk_bf16(acc[0], acc[1]), ta, pk_bf16(acc[2], acc[3]), tb});
    }
    asm volatile("" ::"v"(pf));
    while ((unsigned)__builtin_amdgcn_s_memrealtime() - t0 < (unsigned)P_TPOLL) __builtin_amdgcn_s_sleep(4);
    {
      const u32x4 tu = ld_unit(rws, (l & 7) * 128, WS_TBOX);
      u32x4 qa = ld_unit(rws, l16, WS_QBOX + (kvh * 16 + b) * 1024);
      int d = bad_unit(tu, ta, tb) == 0u ? (int)(tu.x - t0) : 0;
      d = min(max(d, -300), 300);
      d += __builtin_amdgcn_mov_dpp(d, XOR1, 0xf, 0xf, true); d += __builtin_amdgcn_mov_dpp(d, XOR2, 0xf, 0xf, true);
      d += __builtin_amdgcn_mov_dpp(d, XOR7, 0xf, 0xf, true); d >>= 3;
      *(LAS unsigned*)(L + L_TREF) = t0 + (unsigned)d;
      for (unsigned spins = 0; !__all(bad_unit(qa, ta, tb) == 0u) && spins < SPIN_CAP; ++spins) {
        __builtin_amdgcn_s_sleep(8);
        CFENCE();
        qa = ld_unit(rws, l16, WS_QBOX + (kvh * 16 + b) * 1024);
      }
      *(LAS u32x2*)(L + L_Q + l * 8) = u32x2{qa.x, qa.z};
      asm volatile("s_waitcnt lgkmcnt(0)" ::: "memory");
      *(volatile LAS unsigned*)(L + L_FLAG) = 1u;
    }
    __builtin_amdgcn_s_setprio(0);
    for (unsigned spins = 0; !__all(*(volatile LAS unsigned*)(L + L_MLF + (l & 7) * 4) != 0u) && spins < (SPIN_CAP << 4); ++spins) __builtin_amdgcn_s_sleep(16);
    CFENCE();
    float fw[8];
    {
      const int h = l >> 5;
      float mw[8], lw[8];
#pragma unroll
      for (int ww = 0; ww < 8; ++ww) { mw[ww] = *(LAS float*)(L + L_ML + ww * 16 + h * 4); lw[ww] = *(LAS float*)(L + L_ML + ww * 16 + 8 + h * 4); }
      float M = mw[0];
#pragma unroll
      for (int ww = 1; ww < 8; ++ww) M = fmaxf(M, mw[ww]);
      float Ls = 0.f;
#pragma unroll
      for (int ww = 0; ww < 8; ++ww) { fw[ww] = __builtin_amdgcn_exp2f(mw[ww] - M); Ls = fmaf(fw[ww], lw[ww], Ls); }
      const float inv = 1.0f / Ls;
#pragma unroll
      for (int ww = 0; ww < 8; ++ww) { fw[ww] *= inv; asm volatile("" : "+v"(fw[ww])); }
    }
    int ooff = WS_OBOX + (kvh * 16 + b) * 1024 + l16;
    asm volatile("" : "+v"(ooff));
    __builtin_amdgcn_s_setprio(3);
    BARRIER();
    f32x4 ow[8];
#pragma unroll
    for (int ww = 0; ww < 8; ++ww) ow[ww] = *(LAS f32x4*)(L + L_RED + ww * 1024 + l16);
    const f32x4 O = ((fw[0] * ow[0] + fw[1] * ow[1]) + (fw[2] * ow[2] + fw[3] * ow[3])) + ((fw[4] * ow[4] + fw[5] * ow[5]) + (fw[6] * ow[6] + fw[7] * ow[7]));
    st_unit(rws, ooff, u32x4{pk_bf16(O.x, O.y), ta, pk_bf16(O.z, O.w), tb});
    if (oproj) BARRIER();
  } else {
    const __amdgpu_buffer_rsrc_t rk = make_rsrc(kc, B * HKV * SK * DH * 2);
    const __amdgpu_buffer_rsrc_t rv = make_rsrc(vc, B * HKV * SK * DH * 2);
    unsigned tp0 = t0 + (unsigned)(P_PACE0 + ((sw * P_PACE) >> 5));
    u32x4 R[64];
    const __amdgpu_buffer_rsrc_t rwo = make_rsrc(wo, NQ * D * 2);
    u32x4 wor[4];
    constexpr auto item = [](int j) constexpr { return j == 0 ? 0 : (j == 1 ? 32 : (j <= 32 ? j - 1 : j)); };
    constexpr auto pos = [](int i) constexpr { return i == 0 ? 0 : (i == 32 ? 1 : (i < 32 ? i + 1 : i)); };
#define ISSUE(j) do { constexpr int it_ = item(j);                                                                               \
      if constexpr ((j) >= W0) { wait_vm<WIN - 1>(); pace_until(tp0 + (unsigned)due(j)); SB(); }                                 \
      R[it_] = __builtin_amdgcn_raw_buffer_load_b128(it_ < 32 ? rk : rv, offa, (it_ * 8192 + rot8k) & 0x3ffff, AUX_NT); SB(); } while (0)
    auto phase_a = [&](auto self, auto jc) {
      constexpr int j = decltype(jc)::value;
      if constexpr (j < KB) {
        ISSUE(j);
        if constexpr (j == 1) {
#pragma unroll
          for (int u = 0; u < 4; ++u) { wor[u] = __builtin_amdgcn_raw_buffer_load_b128(rwo, offb, u * 8 * D * 2, AUX_NT); SB(); }
        }
        if constexpr (j == W0 - 1) { BARRIER(); EPOCH_GET(); }
        if constexpr (j == W0) {
#pragma unroll
          for (int u = 0; u < 4; ++u) *(LAS u32x4*)(L + L_WO + (sw * 32 + u * 8 + (l >> 3)) * ROWP + (l & 7) * 16) = wor[u];
          SB();
        }
        self(self, std::integral_constant<int, j + 1>{});
      }
    };
    phase_a(phase_a, std::integral_constant<int, 0>{});
    asm volatile("" ::"v"(pf));
    for (unsigned spins = 0; *(volatile LAS unsigned*)(L + L_FLAG) == 0u && spins < (SPIN_CAP << 4); ++spins) __builtin_amdgcn_s_sleep(1);
    CFENCE();
    {
      const int dt = (int)((unsigned)__builtin_amdgcn_readfirstlane(*(LAS unsigned*)(L + L_TREF)) - t0);
      tp0 = t0 + (unsigned)(min(max(dt, -400), 400) + P_PACE1 + ((sw * P_PACE) >> 5));
    }
    const u32x4 q0 = *(LAS u32x4*)(L + L_Q + c * 16), q1 = *(LAS u32x4*)(L + L_Q + 256 + c * 16);
    const bool up8 = (c & 8) != 0, up4 = (c & 4) != 0, up2 = (c & 2) != 0, up1 = (c & 1) != 0;
    float u0[4], u1[4], sc[2][2];
    const int hh = c >> 3, jj = c & 7;
    const unsigned pick = (unsigned)((2 * hh + 1) << 8 | (2 * hh));
    const unsigned act = (jj & 1) ? (pick << 16 | 0x0C0Cu) : (0x0C0C0000u | pick);
    u32x4 sel;
    sel.x = (jj >> 1) == 0 ? act : 0x0C0C0C0Cu; sel.y = (jj >> 1) == 1 ? act : 0x0C0C0C0Cu;
    sel.z = (jj >> 1) == 2 ? act : 0x0C0C0C0Cu; sel.w = (jj >> 1) == 3 ? act : 0x0C0C0C0Cu;
    unsigned P0 = 0, P1 = 0;
    f32x4 acc = {0.f, 0.f, 0.f, 0.f};
    u32x4 afr = {0u, 0u, 0u, 0u};
    constexpr float C2 = 0.088388347648318440f * 1.4426950408889634f;
#define COMB(UP, CTRL, LO, HI) ((UP ? HI : LO) + dpp<CTRL>(UP ? LO : HI))
#define AFRAG(I) do { const unsigned t_ = dppu<BCAST + bitrev4((I) & 15)>((I) < 16 ? P0 : P1);                                   \
      afr.x = __builtin_amdgcn_perm(t_, t_, sel.x); afr.y = __builtin_amdgcn_perm(t_, t_, sel.y);                                \
      afr.z = __builtin_amdgcn_perm(t_, t_, sel.z); afr.w = __builtin_amdgcn_perm(t_, t_, sel.w); } while (0)
    auto process = [&](auto ic) {
      constexpr int i = decltype(ic)::value;
      if constexpr (i < 32) {
        const u32x4 kk = R[i];
        float a = dot2(kk.x, q0.x, 0.f), bb = dot2(kk.x, q1.x, 0.f);
        a = dot2(kk.y, q0.y, a); bb = dot2(kk.y, q1.y, bb);
        a = dot2(kk.z, q0.z, a); bb = dot2(kk.z, q1.z, bb);
        a = dot2(kk.w, q0.w, a); bb = dot2(kk.w, q1.w, bb);
        if constexpr (!(i & 1)) { u0[0] = a; u1[0] = bb; }
        else {
          a = COMB(up8, XOR8, u0[0], a); bb = COMB(up8, XOR8, u1[0], bb);
          if constexpr (!(i & 2)) { u0[1] = a; u1[1] = bb; }
          else {
            a = COMB(up4, XOR7, u0[1], a); bb = COMB(up4, XOR7, u1[1], bb);
            if constexpr (!(i & 4)) { u0[2] = a; u1[2] = bb; }
            else {
              a = COMB(up2, XOR2, u0[2], a); bb = COMB(up2, XOR2, u1[2], bb);
              if constexpr (!(i & 8)) { u0[3] = a; u1[3] = bb; }
              else {
                a = COMB(up1, XOR1, u0[3], a); bb = COMB(up1, XOR1, u1[3], bb);
                sc[i >> 4][0] = a; sc[i >> 4][1] = bb;
              }
            }
          }
        }
        if constexpr (i == 31) {
          float m[2], lsum[2], p[2][2];
#pragma unroll
          for (int h = 0; h < 2; ++h) {
            const float s0 = sc[0][h] * C2, s1 = sc[1][h] * C2;
            m[h] = wave_max(fmaxf(s0, s1));
            p[0][h] = __builtin_amdgcn_exp2f(s0 - m[h]); p[1][h] = __builtin_amdgcn_exp2f(s1 - m[h]);
            lsum[h] = wave_sum(p[0][h] + p[1][h]);
          }
          P0 = pk_bf16(p[0][0], p[0][1]); P1 = pk_bf16(p[1][0], p[1][1]);
          if (l == 0) {
            *(LAS f32x4*)(L + L_ML + sw * 16) = f32x4{m[0], m[1], lsum[0], lsum[1]};
            CFENCE();
            *(volatile LAS unsigned*)(L + L_MLF + sw * 4) = 1u;
          }
          AFRAG(0);
        }
      } else {
        acc = mfma16(afr, R[i], acc);
        if constexpr (i < 63) AFRAG(i - 31);
      }
      SB();
    };
    auto phase_b = [&](auto self, auto jc, auto dc) {
      constexpr int j = decltype(jc)::value, done = decltype(dc)::value;
      if constexpr (done < 64) {
        constexpr int landed = j < 64 ? j - WIN : 64;
        auto run = [&](auto self2, auto ic) {
          constexpr int i = decltype(ic)::value;
          if constexpr (i < 64 && i < done + CHUNK && pos(i < 64 ? i : 0) < landed) { process(ic); return self2(self2, std::integral_constant<int, i + 1>{}); }
          else return ic;
        };
        auto nd = run(run, std::integral_constant<int, done>{});
        if constexpr (j < 64) ISSUE(j);
        self(self, std::integral_constant<int, (j < 64 ? j + 1 : 64)>{}, nd);
      }
    };
    phase_b(phase_b, std::integral_constant<int, KB>{}, std::integral_constant<int, 0>{});
#undef COMB
#undef AFRAG
#undef ISSUE
    *(LAS f32x4*)(L + L_RED + sw * 1024 + (g >> 1) * 512 + c * 32 + (g & 1) * 16) = acc;
    BARRIER();
    if (oproj) {
      const int off = (c & 7) * 1024 + sw * 128 + g * 32 + (c >> 3) * 16;
      __builtin_amdgcn_s_sleep(P_D1);
      SB();
      u32x4 a0 = ld_unit(rws, off, WS_OBOX + kvh * 16384), a1 = ld_unit(rws, off, WS_OBOX + kvh * 16384 + 8192);
      SB();
      u32x4 wfr[4];
#pragma unroll
      for (int t = 0; t < 4; ++t) wfr[t] = tr_frag(L + L_WO + (sw * 32 + 8 * g + (c >> 2)) * ROWP + t * 32 + (c & 3) * 8, 4 * ROWP);
      SB();
      {
        bool ok = (bad_unit(a0, ta, tb) | bad_unit(a1, ta, tb)) == 0u;
        for (unsigned spins = 0; !__all(ok) && spins < SPIN_CAP; ++spins) {
          CFENCE();
          if (!ok) {
            a0 = ld_unit(rws, off, WS_OBOX + kvh * 16384);
            a1 = ld_unit(rws, off, WS_OBOX + kvh * 16384 + 8192);
            ok = (bad_unit(a0, ta, tb) | bad_unit(a1, ta, tb)) == 0u;
          }
        }
      }
      const bool lo8 = c < 8;
      const unsigned gx = dppu<XOR8>(lo8 ? a1.x : a0.x), gz = dppu<XOR8>(lo8 ? a1.z : a0.z);
      const u32x4 of = {lo8 ? a0.x : gx, lo8 ? a0.z : gz, lo8 ? gx : a1.x, lo8 ? gz : a1.z};
#pragma unroll
      for (int t = 0; t < 4; ++t) {
        const f32x4 d = mfma16(wfr[t], of, f32x4{0.f, 0.f, 0.f, 0.f});
        *(LAS f32x4*)(L + L_RED2 + (sw * 4 + t) * 1024 + l16) = d;
      }
      BARRIER();
      if (sw < 4) {
        f32x4 s = *(LAS f32x4*)(L + L_RED2 + sw * 1024 + l16);
#pragma unroll
        for (int ww = 1; ww < 8; ++ww) s += *(LAS f32x4*)(L + L_RED2 + (ww * 4 + sw) * 1024 + l16);
        const bool lo = c < 8;
        const unsigned v0 = __float_as_uint(s.x), v1 = __float_as_uint(s.y), n0 = dppu<XOR8>(__float_as_uint(s.z)), n1 = dppu<XOR8>(__float_as_uint(s.w));
        const int base = WS_PBOX + (((c & 7) * 16 + kvh) * 288 + b * 32 + sw * 8 + g * 2) * 16;
        st_unit(rws, base + (lo ? 0 : 16), u32x4{lo ? v0 : n0, ta, lo ? v1 : n1, tb});
        st_unit(rws, base + 8 * 16 * 288 * 16 + (lo ? 16 : 0), u32x4{lo ? n0 : v0, ta, lo ? n1 : v1, tb});
      } else {
        const int poff = ((kvh * 16 + (l >> 3)) * 288 + b * 32 + (sw - 4) * 8 + (l & 7)) * 16;
        __builtin_amdgcn_s_sleep(P_D2);
        u32x4 p0 = ld_unit(rws, poff, WS_PBOX), p1 = ld_unit(rws, poff + 8 * 288 * 16, WS_PBOX);
        bool ok0 = bad_unit(p0, ta, tb) == 0u, ok1 = bad_unit(p1, ta, tb) == 0u;
        for (unsigned spins = 0; !__all(ok0 && ok1) && spins < SPIN_CAP; ++spins) {
          CFENCE();
          if (!ok0) p0 = ld_unit(rws, poff, WS_PBOX);
          if (!ok1) p1 = ld_unit(rws, poff + 8 * 288 * 16, WS_PBOX);
          ok0 = bad_unit(p0, ta, tb) == 0u; ok1 = bad_unit(p1, ta, tb) == 0u;
        }
        float v0 = __uint_as_float(p0.x) + __uint_as_float(p1.x), v1 = __uint_as_float(p0.z) + __uint_as_float(p1.z);
        v0 += dpp<XOR8>(v0); v1 += dpp<XOR8>(v1);
        v0 = xrow_sum(v0); v1 = xrow_sum(v1);
        if (l < 8) *(unsigned*)(out + kvh * D + b * 64 + (sw - 4) * 16 + l * 2) = pk_bf16(v0, v1);
        if (bid == 0 && tid == 5 * 64) __builtin_amdgcn_raw_buffer_store_b64(u32x2{ta, (unsigned)(e1 >> 32)}, rws, 0, WS_EPOCH, AUX_SC1);
      }
    }
  }
  asm volatile(".Lfused_code_end:");
}

extern "C" void kernel_launch(void* const* d_in, const int* in_sizes, int n_in, void* d_out, int out_size, void* d_ws,
                              size_t ws_size, hipStream_t stream) {
  fused_kernel<<<256, NWAVES * 64, 0, stream>>>((const unsigned short*)d_in[0], (const unsigned short*)d_in[1], (const unsigned short*)d_in[2],
                                                (const unsigned short*)d_in[3], (const unsigned short*)d_in[4], (unsigned short*)d_out,
                                                (unsigned char*)d_ws);
}
```

```cpp
#define LIM 420
#include <hip/hip_runtime.h>
#include <hip/hip_bf16.h>
#include <cstdint>
#include <type_traits>

#define DIAG 0
#define P_PACE 118
#define P_PACE0 185
#define P_PACE1 170
#define P_TPOLL 660
#define P_D1 14
#define P_D2 25

#define P_J1 64
#define P_PACEB 120
constexpr int due(int j) { return (j <= P_J1 ? j * P_PACE : P_J1 * P_PACE + (j - P_J1) * P_PACEB) >> 2; }
constexpr int B = 16, D = 576, HQ = 32, HKV = 16, SK = 4096, DH = 128, W = 1024;
constexpr int NQ = HQ * DH;
constexpr int NWAVES = 9;
constexpr int W0 = 4;
constexpr int WIN = 6;
constexpr int KB = 30;
constexpr int CHUNK = 3;

typedef unsigned u32x4 __attribute__((ext_vector_type(4)));
typedef unsigned u32x2 __attribute__((ext_vector_type(2)));
typedef float f32x4 __attribute__((ext_vector_type(4)));
typedef short bf16x8 __attribute__((ext_vector_type(8)));
typedef short s16x4 __attribute__((ext_vector_type(4)));
typedef __bf16 bf16x2 __attribute__((ext_vector_type(2)));
#define LAS __attribute__((address_space(3)))

constexpr int AUX_SC1 = 16, AUX_NT = 2;
constexpr int RSRC_W3 = 0x00020000;

constexpr int WS_EPOCH = 0;
constexpr int WS_TBOX = 1024;
constexpr int WS_QBOX = 4096;
constexpr int WS_OBOX = WS_QBOX + 16 * 16 * 1024;
constexpr int WS_PBOX = WS_OBOX + 16 * 16 * 1024;
constexpr int WS_END = WS_PBOX + 16 * 16 * 288 * 16;
constexpr int WS_DIAG = 8 << 20;
constexpr unsigned SPIN_CAP = 1u << 18;
constexpr int bitrev4(int i) { return ((i & 1) << 3) | ((i & 2) << 1) | ((i & 4) >> 1) | ((i & 8) >> 3); }

constexpr int ROWP = 144;
constexpr int L_RED = 0;
constexpr int L_ML = 8192;
constexpr int L_Q = L_ML + 128;
constexpr int L_FLAG = L_Q + 512;
constexpr int L_TREF = L_FLAG + 4;
constexpr int L_MLF = L_FLAG + 16;
constexpr int L_RED2 = 9216;
constexpr int L_WQ = L_RED2;
constexpr int L_WO = L_RED2 + 32768;
constexpr int L_BYTES = L_WO + 256 * ROWP;

__device__ __forceinline__ float dot2(unsigned k, unsigned q, float acc) {
  return __builtin_amdgcn_fdot2_f32_bf16(__builtin_bit_cast(bf16x2, k), __builtin_bit_cast(bf16x2, q), acc, false);
}
__device__ __forceinline__ unsigned pk_bf16(float lo, float hi) {
  bf16x2 v; v.x = (__bf16)lo; v.y = (__bf16)hi;
  return __builtin_bit_cast(unsigned, v);
}
template <int CTRL> __device__ __forceinline__ float dpp(float x) {
  return __builtin_bit_cast(float, __builtin_amdgcn_mov_dpp(__builtin_bit_cast(int, x), CTRL, 0xf, 0xf, true));
}
template <int CTRL> __device__ __forceinline__ unsigned dppu(unsigned x) {
  return (unsigned)__builtin_amdgcn_mov_dpp((int)x, CTRL, 0xf, 0xf, true);
}
constexpr int XOR1 = 0xB1, XOR2 = 0x4E, XOR7 = 0x141, XOR8 = 0x128, BCAST = 0x150;
__device__ __forceinline__ float xrow_max(float x) {
  auto s = __builtin_amdgcn_permlane16_swap(__float_as_uint(x), __float_as_uint(x), false, false);
  x = fmaxf(__uint_as_float(s[0]), __uint_as_float(s[1]));
  auto t = __builtin_amdgcn_permlane32_swap(__float_as_uint(x), __float_as_uint(x), false, false);
  return fmaxf(__uint_as_float(t[0]), __uint_as_float(t[1]));
}
__device__ __forceinline__ float xrow_sum(float x) {
  auto s = __builtin_amdgcn_permlane16_swap(__float_as_uint(x), __float_as_uint(x), false, false);
  x = __uint_as_float(s[0]) + __uint_as_float(s[1]);
  auto t = __builtin_amdgcn_permlane32_swap(__float_as_uint(x), __float_as_uint(x), false, false);
  return __uint_as_float(t[0]) + __uint_as_float(t[1]);
}
__device__ __forceinline__ float wave_max(float x) {
  x = fmaxf(x, dpp<XOR1>(x)); x = fmaxf(x, dpp<XOR2>(x)); x = fmaxf(x, dpp<XOR7>(x)); x = fmaxf(x, dpp<XOR8>(x));
  return xrow_max(x);
}
__device__ __forceinline__ float wave_sum(float x) {
  x += dpp<XOR1>(x); x += dpp<XOR2>(x); x += dpp<XOR7>(x); x += dpp<XOR8>(x);
  return xrow_sum(x);
}
__device__ __forceinline__ f32x4 mfma16(u32x4 a, u32x4 b, f32x4 c) {
  return __builtin_amdgcn_mfma_f32_16x16x32_bf16(__builtin_bit_cast(bf16x8, a), __builtin_bit_cast(bf16x8, b), c, 0, 0, 0);
}
__device__ __forceinline__ __amdgpu_buffer_rsrc_t make_rsrc(const void* p, int bytes) {
  return __builtin_amdgcn_make_buffer_rsrc((void*)p, 0, bytes, RSRC_W3);
}
__device__ __forceinline__ u32x4 tr_frag(LAS unsigned char* p, int second) {
  s16x4 lo = __builtin_amdgcn_ds_read_tr16_b64_v4i16((LAS s16x4*)p);
  s16x4 hi = __builtin_amdgcn_ds_read_tr16_b64_v4i16((LAS s16x4*)(p + second));
  u32x2 a = __builtin_bit_cast(u32x2, lo), b = __builtin_bit_cast(u32x2, hi);
  return u32x4{a.x, a.y, b.x, b.y};
}
__device__ __forceinline__ unsigned bad_unit(u32x4 v, unsigned ta, unsigned tb) { return (v.y ^ ta) | (v.w ^ tb); }
__device__ __forceinline__ void st_unit(__amdgpu_buffer_rsrc_t r, int off, u32x4 v) { __builtin_amdgcn_raw_buffer_store_b128(v, r, off, 0, AUX_SC1); }
__device__ __forceinline__ u32x4 ld_unit(__amdgpu_buffer_rsrc_t r, int off, int soff) { return __builtin_amdgcn_raw_buffer_load_b128(r, off, soff, AUX_SC1); }
#define SB() __builtin_amdgcn_sched_barrier(0)
#define CFENCE() asm volatile("" ::: "memory")
#define BARRIER() do { CFENCE(); asm volatile("s_waitcnt lgkmcnt(0)" ::: "memory"); __builtin_amdgcn_s_barrier(); CFENCE(); SB(); } while (0)
template <int N> __device__ __forceinline__ void wait_vm() { asm volatile("s_waitcnt vmcnt(%0)" ::"n"(N) : "memory"); }
__device__ __forceinline__ void pace_until(unsigned due) {
  asm volatile("1:\n\ts_memrealtime s[96:97]\n\ts_waitcnt lgkmcnt(0)\n\ts_sub_u32 s96, s96, %0\n\ts_cmp_lt_i32 s96, 0\n\ts_cbranch_scc1 1b"
               :: "s"(due) : "s96", "s97", "scc", "memory");
}

#define STAMP(i) do { } while (0)

__global__ void __launch_bounds__(NWAVES * 64, 3)
fused_kernel(const unsigned short* __restrict__ x, const unsigned short* __restrict__ kc, const unsigned short* __restrict__ vc,
             const unsigned short* __restrict__ wq, const unsigned short* __restrict__ wo, unsigned short* __restrict__ out,
             unsigned char* __restrict__ ws) {
  __shared__ __attribute__((aligned(16))) unsigned char smem[L_BYTES];
  LAS unsigned char* L = (LAS unsigned char*)smem;
  const int bid = blockIdx.x;
  const int kvh = (bid & 7) * 2 + ((bid >> 3) & 1), b = bid >> 4;
  const int tid = threadIdx.x, l = tid & 63, w = __builtin_amdgcn_readfirstlane(tid >> 6), g = l >> 4, c = l & 15;
  const int sw = w - 1;
  STAMP(0);
  unsigned pf;
  {
    unsigned long long pc; unsigned clen;
    asm volatile("s_getpc_b64 %0\n.Lfused_code_begin:\n\ts_mov_b32 %1, .Lfused_code_end-.Lfused_code_begin" : "=s"(pc), "=s"(clen));
    pf = __builtin_amdgcn_raw_buffer_load_b32(make_rsrc((const void*)pc, (int)clen), tid < 8 ? (tid * 32 + (bid >> 3)) * 128 : 0x7fff0000, 0, 0);
  }
  unsigned long long t64 = __builtin_amdgcn_s_memrealtime();
  *(LAS unsigned*)(L + L_FLAG + (l & 15) * 4) = 0u;
  const bool oproj = b < 9;
  const int offa_s = ((b * HKV + kvh) * SK + (SK - W) - 4) * (DH * 2) + tid * 16, offa_q = (l >> 1) * (NQ * 2) + (l & 1) * 16 + (kvh * 256 + b * 16) * 2;
  const int offb_s = (kvh * 256 - 32 + (tid >> 6) * 32 + (l >> 3)) * (D * 2) + (oproj ? b * 128 : 0x7f000000) + (l & 7) * 16, offb_q = c * (D * 2) + g * 16;
  int offa = tid < 64 ? offa_q : offa_s, offb = tid < 64 ? offb_q : offb_s;
  const int rot8k = (bid >> 3) * 8192;
  asm volatile("" : "+v"(offa), "+v"(offb));
  SB();
  asm volatile("" : "+s"(t64) : "s"(x), "s"(kc), "s"(vc), "s"(wq), "s"(wo), "s"(out), "s"(ws));
  const unsigned t0 = (unsigned)t64;
  const __amdgpu_buffer_rsrc_t rws = make_rsrc(ws, WS_END);
  const int l16 = l * 16;
  unsigned long long e1;
  unsigned ta, tb;
#define EPOCH_GET() do { asm volatile("s_load_dwordx2 %0, %1, 0x0\n\ts_waitcnt lgkmcnt(0)" : "=s"(e1) : "s"(ws + WS_EPOCH) : "memory"); SB(); \
    e1 += 1ull; ta = (unsigned)e1; tb = (unsigned)(e1 >> 32) ^ (ta * 0x9E3779B1u); } while (0)

  if (w == 0) {
    __builtin_amdgcn_s_setprio(3);
    {
      const __amdgpu_buffer_rsrc_t rwq = make_rsrc(wq, D * NQ * 2);
      const __amdgpu_buffer_rsrc_t rx = make_rsrc(x, B * D * 2);
      u32x4 wr[18], xr[18];
#pragma unroll
      for (int s = 0; s < 18; ++s) {
        wr[s] = __builtin_amdgcn_raw_buffer_load_b128(rwq, offa, s * 32 * NQ * 2, AUX_NT); SB();
        xr[s] = __builtin_amdgcn_raw_buffer_load_b128(rx, offb + s * 64, 0, 0); SB();
        if (s == 2) BARRIER();
      }
      EPOCH_GET();
      if (bid < 8 && l < 8) st_unit(rws, WS_TBOX + bid * 128 + l16, u32x4{t0, ta, t0, tb});
      STAMP(1);
#pragma unroll
      for (int s = 0; s < 18; ++s) *(LAS u32x4*)(L + L_WQ + s * 1024 + l16) = wr[s];
      f32x4 acc = {0.f, 0.f, 0.f, 0.f};
      const int troff = (8 * g + (c >> 2)) * 32 + (c & 3) * 8;
#pragma unroll
      for (int s = 0; s < 18; ++s) acc = mfma16(tr_frag(L + L_WQ + s * 1024 + troff, 128), xr[s], acc);
      STAMP(2);
      st_unit(rws, WS_QBOX + kvh * 16384 + c * 1024 + (b * 4 + g) * 16, u32x4{pk_bf16(acc[0], acc[1]), ta, pk_bf16(acc[2], acc[3]), tb});
      STAMP(3);
    }
    asm volatile("" ::"v"(pf));
    while ((unsigned)__builtin_amdgcn_s_memrealtime() - t0 < (unsigned)P_TPOLL) __builtin_amdgcn_s_sleep(4);
    {
      const u32x4 tu = ld_unit(rws, (l & 7) * 128, WS_TBOX);
      u32x4 qa = ld_unit(rws, l16, WS_QBOX + (kvh * 16 + b) * 1024);
      int d = bad_unit(tu, ta, tb) == 0u ? (int)(tu.x - t0) : 0;
      d = min(max(d, -300), 300);
      d += __builtin_amdgcn_mov_dpp(d, XOR1, 0xf, 0xf, true); d += __builtin_amdgcn_mov_dpp(d, XOR2, 0xf, 0xf, true);
      d += __builtin_amdgcn_mov_dpp(d, XOR7, 0xf, 0xf, true); d >>= 3;
      *(LAS unsigned*)(L + L_TREF) = t0 + (unsigned)d;
      for (unsigned spins = 0; !__all(bad_unit(qa, ta, tb) == 0u) && spins < SPIN_CAP; ++spins) {
        __builtin_amdgcn_s_sleep(8);
        CFENCE();
        qa = ld_unit(rws, l16, WS_QBOX + (kvh * 16 + b) * 1024);
      }
      *(LAS u32x2*)(L + L_Q + l * 8) = u32x2{qa.x, qa.z};
      asm volatile("s_waitcnt lgkmcnt(0)" ::: "memory");
      *(volatile LAS unsigned*)(L + L_FLAG) = 1u;
    }
    STAMP(4);
    __builtin_amdgcn_s_setprio(0);
    for (unsigned spins = 0; !__all(*(volatile LAS unsigned*)(L + L_MLF + (l & 7) * 4) != 0u) && spins < (SPIN_CAP << 4); ++spins) __builtin_amdgcn_s_sleep(16);
    CFENCE();
    float fw[8];
    {
      const int h = l >> 5;
      float mw[8], lw[8];
#pragma unroll
      for (int ww = 0; ww < 8; ++ww) { mw[ww] = *(LAS float*)(L + L_ML + ww * 16 + h * 4); lw[ww] = *(LAS float*)(L + L_ML + ww * 16 + 8 + h * 4); }
      float M = mw[0];
#pragma unroll
      for (int ww = 1; ww < 8; ++ww) M = fmaxf(M, mw[ww]);
      float Ls = 0.f;
#pragma unroll
      for (int ww = 0; ww < 8; ++ww) { fw[ww] = __builtin_amdgcn_exp2f(mw[ww] - M); Ls = fmaf(fw[ww], lw[ww], Ls); }
      const float inv = 1.0f / Ls;
#pragma unroll
      for (int ww = 0; ww < 8; ++ww) { fw[ww] *= inv; asm volatile("" : "+v"(fw[ww])); }
    }
    int ooff = WS_OBOX + (kvh * 16 + b) * 1024 + l16;
    asm volatile("" : "+v"(ooff));
    __builtin_amdgcn_s_setprio(3);
    BARRIER();
    STAMP(6);
    f32x4 ow[8];
#pragma unroll
    for (int ww = 0; ww < 8; ++ww) ow[ww] = *(LAS f32x4*)(L + L_RED + ww * 1024 + l16);
    const f32x4 O = ((fw[0] * ow[0] + fw[1] * ow[1]) + (fw[2] * ow[2] + fw[3] * ow[3])) + ((fw[4] * ow[4] + fw[5] * ow[5]) + (fw[6] * ow[6] + fw[7] * ow[7]));
    st_unit(rws, ooff, u32x4{pk_bf16(O.x, O.y), ta, pk_bf16(O.z, O.w), tb});
    STAMP(7);
    if (oproj) BARRIER();
  } else {
    const __amdgpu_buffer_rsrc_t rk = make_rsrc(kc, B * HKV * SK * DH * 2);
    const __amdgpu_buffer_rsrc_t rv = make_rsrc(vc, B * HKV * SK * DH * 2);
    unsigned tp0 = t0 + (unsigned)(P_PACE0 + ((sw * P_PACE) >> 5));
    u32x4 R[64];
    const __amdgpu_buffer_rsrc_t rwo = make_rsrc(wo, NQ * D * 2);
    u32x4 wor[4];
    constexpr auto item = [](int j) constexpr { return j == 0 ? 0 : (j == 1 ? 32 : (j <= 32 ? j - 1 : j)); };
    constexpr auto pos = [](int i) constexpr { return i == 0 ? 0 : (i == 32 ? 1 : (i < 32 ? i + 1 : i)); };
#define ISSUE(j) do { constexpr int it_ = item(j);                                                                               \
      if constexpr ((j) >= W0) { wait_vm<WIN - 1>(); pace_until(tp0 + (unsigned)due(j)); SB(); }                                 \
      R[it_] = __builtin_amdgcn_raw_buffer_load_b128(it_ < 32 ? rk : rv, offa, (it_ * 8192 + rot8k) & 0x3ffff, AUX_NT); SB(); } while (0)
    auto phase_a = [&](auto self, auto jc) {
      constexpr int j = decltype(jc)::value;
      if constexpr (j < KB) {
        ISSUE(j);
        if constexpr (j == 1) {
#pragma unroll
          for (int u = 0; u < 4; ++u) { wor[u] = __builtin_amdgcn_raw_buffer_load_b128(rwo, offb, u * 8 * D * 2, AUX_NT); SB(); }
        }
        if constexpr (j == W0 - 1) { BARRIER(); EPOCH_GET(); }
        if constexpr (j == W0) {
#pragma unroll
          for (int u = 0; u < 4; ++u) *(LAS u32x4*)(L + L_WO + (sw * 32 + u * 8 + (l >> 3)) * ROWP + (l & 7) * 16) = wor[u];
          SB();
        }
        self(self, std::integral_constant<int, j + 1>{});
      }
    };
    phase_a(phase_a, std::integral_constant<int, 0>{});
    STAMP(1);
    asm volatile("" ::"v"(pf));
    for (unsigned spins = 0; *(volatile LAS unsigned*)(L + L_FLAG) == 0u && spins < (SPIN_CAP << 4); ++spins) __builtin_amdgcn_s_sleep(1);
    CFENCE();
    STAMP(4);
    {
      const int dt = (int)((unsigned)__builtin_amdgcn_readfirstlane(*(LAS unsigned*)(L + L_TREF)) - t0);
      tp0 = t0 + (unsigned)(min(max(dt, -400), 400) + P_PACE1 + ((sw * P_PACE) >> 5));
    }
    const u32x4 q0 = *(LAS u32x4*)(L + L_Q + c * 16), q1 = *(LAS u32x4*)(L + L_Q + 256 + c * 16);
    const bool up8 = (c & 8) != 0, up4 = (c & 4) != 0, up2 = (c & 2) != 0, up1 = (c & 1) != 0;
    float u0[4], u1[4], sc[2][2];
    const int hh = c >> 3, jj = c & 7;
    const unsigned pick = (unsigned)((2 * hh + 1) << 8 | (2 * hh));
    const unsigned act = (jj & 1) ? (pick << 16 | 0x0C0Cu) : (0x0C0C0000u | pick);
    u32x4 sel;
    sel.x = (jj >> 1) == 0 ? act : 0x0C0C0C0Cu; sel.y = (jj >> 1) == 1 ? act : 0x0C0C0C0Cu;
    sel.z = (jj >> 1) == 2 ? act : 0x0C0C0C0Cu; sel.w = (jj >> 1) == 3 ? act : 0x0C0C0C0Cu;
    unsigned P0 = 0, P1 = 0;
    f32x4 acc = {0.f, 0.f, 0.f, 0.f};
    u32x4 afr = {0u, 0u, 0u, 0u};
    constexpr float C2 = 0.088388347648318440f * 1.4426950408889634f;
#define COMB(UP, CTRL, LO, HI) ((UP ? HI : LO) + dpp<CTRL>(UP ? LO : HI))
#define AFRAG(I) do { const unsigned t_ = dppu<BCAST + bitrev4((I) & 15)>((I) < 16 ? P0 : P1);                                   \
      afr.x = __builtin_amdgcn_perm(t_, t_, sel.x); afr.y = __builtin_amdgcn_perm(t_, t_, sel.y);                                \
      afr.z = __builtin_amdgcn_perm(t_, t_, sel.z); afr.w = __builtin_amdgcn_perm(t_, t_, sel.w); } while (0)
    auto process = [&](auto ic) {
      constexpr int i = decltype(ic)::value;
      if constexpr (i < 32) {
        const u32x4 kk = R[i];
        float a = dot2(kk.x, q0.x, 0.f), bb = dot2(kk.x, q1.x, 0.f);
        a = dot2(kk.y, q0.y, a); bb = dot2(kk.y, q1.y, bb);
        a = dot2(kk.z, q0.z, a); bb = dot2(kk.z, q1.z, bb);
        a = dot2(kk.w, q0.w, a); bb = dot2(kk.w, q1.w, bb);
        if constexpr (!(i & 1)) { u0[0] = a; u1[0] = bb; }
        else {
          a = COMB(up8, XOR8, u0[0], a); bb = COMB(up8, XOR8, u1[0], bb);
          if constexpr (!(i & 2)) { u0[1] = a; u1[1] = bb; }
          else {
            a = COMB(up4, XOR7, u0[1], a); bb = COMB(up4, XOR7, u1[1], bb);
            if constexpr (!(i & 4)) { u0[2] = a; u1[2] = bb; }
            else {
              a = COMB(up2, XOR2, u0[2], a); bb = COMB(up2, XOR2, u1[2], bb);
              if constexpr (!(i & 8)) { u0[3] = a; u1[3] = bb; }
              else {
                a = COMB(up1, XOR1, u0[3], a); bb = COMB(up1, XOR1, u1[3], bb);
                sc[i >> 4][0] = a; sc[i >> 4][1] = bb;
              }
            }
          }
        }
        if constexpr (i == 31) {
          float m[2], lsum[2], p[2][2];
#pragma unroll
          for (int h = 0; h < 2; ++h) {
            const float s0 = sc[0][h] * C2, s1 = sc[1][h] * C2;
            m[h] = wave_max(fmaxf(s0, s1));
            p[0][h] = __builtin_amdgcn_exp2f(s0 - m[h]); p[1][h] = __builtin_amdgcn_exp2f(s1 - m[h]);
            lsum[h] = wave_sum(p[0][h] + p[1][h]);
          }
          P0 = pk_bf16(p[0][0], p[0][1]); P1 = pk_bf16(p[1][0], p[1][1]);
          if (l == 0) {
            *(LAS f32x4*)(L + L_ML + sw * 16) = f32x4{m[0], m[1], lsum[0], lsum[1]};
            CFENCE();
            *(volatile LAS unsigned*)(L + L_MLF + sw * 4) = 1u;
          }
          AFRAG(0);
          STAMP(5);
        }
      } else {
        acc = mfma16(afr, R[i], acc);
        if constexpr (i < 63) AFRAG(i - 31);
      }
      SB();
    };
    auto phase_b = [&](auto self, auto jc, auto dc) {
      constexpr int j = decltype(jc)::value, done = decltype(dc)::value;
      if constexpr (done < 64) {
        constexpr int landed = j < 64 ? j - WIN : 64;
        auto run = [&](auto self2, auto ic) {
          constexpr int i = decltype(ic)::value;
          if constexpr (i < 64 && i < done + CHUNK && pos(i < 64 ? i : 0) < landed) { process(ic); return self2(self2, std::integral_constant<int, i + 1>{}); }
          else return ic;
        };
        auto nd = run(run, std::integral_constant<int, done>{});
        if constexpr (j < 64) ISSUE(j);
        self(self, std::integral_constant<int, (j < 64 ? j + 1 : 64)>{}, nd);
      }
    };
    phase_b(phase_b, std::integral_constant<int, KB>{}, std::integral_constant<int, 0>{});
#undef COMB
#undef AFRAG
#undef ISSUE
    *(LAS f32x4*)(L + L_RED + sw * 1024 + (g >> 1) * 512 + c * 32 + (g & 1) * 16) = acc;
    STAMP(6);
    BARRIER();
    if (oproj) {
      const int off = (c & 7) * 1024 + sw * 128 + g * 32 + (c >> 3) * 16;
      __builtin_amdgcn_s_sleep(P_D1);
      SB();
      u32x4 a0 = ld_unit(rws, off, WS_OBOX + kvh * 16384), a1 = ld_unit(rws, off, WS_OBOX + kvh * 16384 + 8192);
      SB();
      u32x4 wfr[4];
#pragma unroll
      for (int t = 0; t < 4; ++t) wfr[t] = tr_frag(L + L_WO + (sw * 32 + 8 * g + (c >> 2)) * ROWP + t * 32 + (c & 3) * 8, 4 * ROWP);
      SB();
      {
        bool ok = (bad_unit(a0, ta, tb) | bad_unit(a1, ta, tb)) == 0u;
        for (unsigned spins = 0; !__all(ok) && spins < SPIN_CAP; ++spins) {
          CFENCE();
          if (!ok) {
            a0 = ld_unit(rws, off, WS_OBOX + kvh * 16384);
            a1 = ld_unit(rws, off, WS_OBOX + kvh * 16384 + 8192);
            ok = (bad_unit(a0, ta, tb) | bad_unit(a1, ta, tb)) == 0u;
          }
        }
      }
      STAMP(8);
      const bool lo8 = c < 8;
      const unsigned gx = dppu<XOR8>(lo8 ? a1.x : a0.x), gz = dppu<XOR8>(lo8 ? a1.z : a0.z);
      const u32x4 of = {lo8 ? a0.x : gx, lo8 ? a0.z : gz, lo8 ? gx : a1.x, lo8 ? gz : a1.z};
#pragma unroll
      for (int t = 0; t < 4; ++t) {
        const f32x4 d = mfma16(wfr[t], of, f32x4{0.f, 0.f, 0.f, 0.f});
        *(LAS f32x4*)(L + L_RED2 + (sw * 4 + t) * 1024 + l16) = d;
      }
      BARRIER();
      if (sw < 4) {
        f32x4 s = *(LAS f32x4*)(L + L_RED2 + sw * 1024 + l16);
#pragma unroll
        for (int ww = 1; ww < 8; ++ww) s += *(LAS f32x4*)(L + L_RED2 + (ww * 4 + sw) * 1024 + l16);
        const bool lo = c < 8;
        const unsigned v0 = __float_as_uint(s.x), v1 = __float_as_uint(s.y), n0 = dppu<XOR8>(__float_as_uint(s.z)), n1 = dppu<XOR8>(__float_as_uint(s.w));
        const int base = WS_PBOX + (((c & 7) * 16 + kvh) * 288 + b * 32 + sw * 8 + g * 2) * 16;
        st_unit(rws, base + (lo ? 0 : 16), u32x4{lo ? v0 : n0, ta, lo ? v1 : n1, tb});
        st_unit(rws, base + 8 * 16 * 288 * 16 + (lo ? 16 : 0), u32x4{lo ? n0 : v0, ta, lo ? n1 : v1, tb});
        STAMP(9);
      } else {
        const int poff = ((kvh * 16 + (l >> 3)) * 288 + b * 32 + (sw - 4) * 8 + (l & 7)) * 16;
        __builtin_amdgcn_s_sleep(P_D2);
        u32x4 p0 = ld_unit(rws, poff, WS_PBOX), p1 = ld_unit(rws, poff + 8 * 288 * 16, WS_PBOX);
        bool ok0 = bad_unit(p0, ta, tb) == 0u, ok1 = bad_unit(p1, ta, tb) == 0u;
        for (unsigned spins = 0; !__all(ok0 && ok1) && spins < SPIN_CAP; ++spins) {
          CFENCE();
          if (!ok0) p0 = ld_unit(rws, poff, WS_PBOX);
          if (!ok1) p1 = ld_unit(rws, poff + 8 * 288 * 16, WS_PBOX);
          ok0 = bad_unit(p0, ta, tb) == 0u; ok1 = bad_unit(p1, ta, tb) == 0u;
        }
        float v0 = __uint_as_float(p0.x) + __uint_as_float(p1.x), v1 = __uint_as_float(p0.z) + __uint_as_float(p1.z);
        v0 += dpp<XOR8>(v0); v1 += dpp<XOR8>(v1);
        v0 = xrow_sum(v0); v1 = xrow_sum(v1);
        if (l < 8) *(unsigned*)(out + kvh * D + b * 64 + (sw - 4) * 16 + l * 2) = pk_bf16(v0, v1);
        if (bid == 0 && tid == 5 * 64) __builtin_amdgcn_raw_buffer_store_b64(u32x2{ta, (unsigned)(e1 >> 32)}, rws, 0, WS_EPOCH, AUX_SC1);
        { const unsigned tn = (unsigned)__builtin_amdgcn_s_memrealtime();
          if ((int)(tn - (tp0 - (unsigned)((sw * P_PACE) >> 5) + (unsigned)due(63))) > LIM) pace_until(tn + 1000u); }
      }
    }
  }
  asm volatile(".Lfused_code_end:");
}

extern "C" void kernel_launch(void* const* d_in, const int* in_sizes, int n_in, void* d_out, int out_size, void* d_ws,
                              size_t ws_size, hipStream_t stream) {
  fused_kernel<<<256, NWAVES * 64, 0, stream>>>((const unsigned short*)d_in[0], (const unsigned short*)d_in[1], (const unsigned short*)d_in[2],
                                                (const unsigned short*)d_in[3], (const unsigned short*)d_in[4], (unsigned short*)d_out,
                                                (unsigned char*)d_ws);
}
```

```cpp
#include <hip/hip_runtime.h>
#include <hip/hip_bf16.h>
#include <cstdint>
#include <type_traits>

constexpr int P_PACE = 118;
constexpr int P_PACE0 = 170;
constexpr int P_PACE1 = 155;
constexpr int P_TPOLL = 645;
constexpr int P_D1 = 14;
constexpr int P_D2 = 25;

constexpr int due(int j) { return (j * P_PACE) >> 2; }

constexpr int B = 16, D = 576, HQ = 32, HKV = 16, SK = 4096, DH = 128, W = 1024;
constexpr int NQ = HQ * DH;
constexpr int NWAVES = 9;
constexpr int W0 = 4;
constexpr int WIN = 6;
constexpr int KB = 30;
constexpr int CHUNK = 3;

typedef unsigned u32x4 __attribute__((ext_vector_type(4)));
typedef unsigned u32x2 __attribute__((ext_vector_type(2)));
typedef float f32x4 __attribute__((ext_vector_type(4)));
typedef short bf16x8 __attribute__((ext_vector_type(8)));
typedef short s16x4 __attribute__((ext_vector_type(4)));
typedef __bf16 bf16x2 __attribute__((ext_vector_type(2)));
#define LAS __attribute__((address_space(3)))

constexpr int AUX_SC1 = 16, AUX_NT = 2;
constexpr int RSRC_W3 = 0x00020000;

constexpr int WS_EPOCH = 0;
constexpr int WS_TBOX = 1024;
constexpr int WS_QBOX = 4096;
constexpr int WS_OBOX = WS_QBOX + 16 * 16 * 1024;
constexpr int WS_PBOX = WS_OBOX + 16 * 16 * 1024;
constexpr int WS_END = WS_PBOX + 16 * 16 * 288 * 16;
constexpr unsigned SPIN_CAP = 1u << 18;
constexpr int bitrev4(int i) { return ((i & 1) << 3) | ((i & 2) << 1) | ((i & 4) >> 1) | ((i & 8) >> 3); }

constexpr int ROWP = 144;
constexpr int L_RED = 0;
constexpr int L_ML = 8192;
constexpr int L_Q = L_ML + 128;
constexpr int L_FLAG = L_Q + 512;
constexpr int L_TREF = L_FLAG + 4;
constexpr int L_MLF = L_FLAG + 16;
constexpr int L_RED2 = 9216;
constexpr int L_WQ = L_RED2;
constexpr int L_WO = L_RED2 + 32768;
constexpr int L_BYTES = L_WO + 256 * ROWP;

__device__ __forceinline__ float dot2(unsigned k, unsigned q, float acc) {
  return __builtin_amdgcn_fdot2_f32_bf16(__builtin_bit_cast(bf16x2, k), __builtin_bit_cast(bf16x2, q), acc, false);
}
__device__ __forceinline__ unsigned pk_bf16(float lo, float hi) {
  bf16x2 v; v.x = (__bf16)lo; v.y = (__bf16)hi;
  return __builtin_bit_cast(unsigned, v);
}
template <int CTRL> __device__ __forceinline__ float dpp(float x) {
  return __builtin_bit_cast(float, __builtin_amdgcn_mov_dpp(__builtin_bit_cast(int, x), CTRL, 0xf, 0xf, true));
}
template <int CTRL> __device__ __forceinline__ unsigned dppu(unsigned x) {
  return (unsigned)__builtin_amdgcn_mov_dpp((int)x, CTRL, 0xf, 0xf, true);
}
constexpr int XOR1 = 0xB1, XOR2 = 0x4E, XOR7 = 0x141, XOR8 = 0x128, BCAST = 0x150;
__device__ __forceinline__ float xrow_max(float x) {
  auto s = __builtin_amdgcn_permlane16_swap(__float_as_uint(x), __float_as_uint(x), false, false);
  x = fmaxf(__uint_as_float(s[0]), __uint_as_float(s[1]));
  auto t = __builtin_amdgcn_permlane32_swap(__float_as_uint(x), __float_as_uint(x), false, false);
  return fmaxf(__uint_as_float(t[0]), __uint_as_float(t[1]));
}
__device__ __forceinline__ float xrow_sum(float x) {
  auto s = __builtin_amdgcn_permlane16_swap(__float_as_uint(x), __float_as_uint(x), false, false);
  x = __uint_as_float(s[0]) + __uint_as_float(s[1]);
  auto t = __builtin_amdgcn_permlane32_swap(__float_as_uint(x), __float_as_uint(x), false, false);
  return __uint_as_float(t[0]) + __uint_as_float(t[1]);
}
__device__ __forceinline__ float wave_max(float x) {
  x = fmaxf(x, dpp<XOR1>(x)); x = fmaxf(x, dpp<XOR2>(x)); x = fmaxf(x, dpp<XOR7>(x)); x = fmaxf(x, dpp<XOR8>(x));
  return xrow_max(x);
}
__device__ __forceinline__ float wave_sum(float x) {
  x += dpp<XOR1>(x); x += dpp<XOR2>(x); x += dpp<XOR7>(x); x += dpp<XOR8>(x);
  return xrow_sum(x);
}
__device__ __forceinline__ f32x4 mfma16(u32x4 a, u32x4 b, f32x4 c) {
  return __builtin_amdgcn_mfma_f32_16x16x32_bf16(__builtin_bit_cast(bf16x8, a), __builtin_bit_cast(bf16x8, b), c, 0, 0, 0);
}
__device__ __forceinline__ __amdgpu_buffer_rsrc_t make_rsrc(const void* p, int bytes) {
  return __builtin_amdgcn_make_buffer_rsrc((void*)p, 0, bytes, RSRC_W3);
}
__device__ __forceinline__ u32x4 tr_frag(LAS unsigned char* p, int second) {
  s16x4 lo = __builtin_amdgcn_ds_read_tr16_b64_v4i16((LAS s16x4*)p);
  s16x4 hi = __builtin_amdgcn_ds_read_tr16_b64_v4i16((LAS s16x4*)(p + second));
  u32x2 a = __builtin_bit_cast(u32x2, lo), b = __builtin_bit_cast(u32x2, hi);
  return u32x4{a.x, a.y, b.x, b.y};
}
__device__ __forceinline__ unsigned bad_unit(u32x4 v, unsigned ta, unsigned tb) { return (v.y ^ ta) | (v.w ^ tb); }
__device__ __forceinline__ void st_unit(__amdgpu_buffer_rsrc_t r, int off, u32x4 v) { __builtin_amdgcn_raw_buffer_store_b128(v, r, off, 0, AUX_SC1); }
__device__ __forceinline__ u32x4 ld_unit(__amdgpu_buffer_rsrc_t r, int off, int soff) { return __builtin_amdgcn_raw_buffer_load_b128(r, off, soff, AUX_SC1); }
#define SB() __builtin_amdgcn_sched_barrier(0)
#define CFENCE() asm volatile("" ::: "memory")
#define BARRIER() do { CFENCE(); asm volatile("s_waitcnt lgkmcnt(0)" ::: "memory"); __builtin_amdgcn_s_barrier(); CFENCE(); SB(); } while (0)
template <int N> __device__ __forceinline__ void wait_vm() { asm volatile("s_waitcnt vmcnt(%0)" ::"n"(N) : "memory"); }
__device__ __forceinline__ void pace_until(unsigned due) {
  asm volatile("1:\n\ts_memrealtime s[96:97]\n\ts_waitcnt lgkmcnt(0)\n\ts_sub_u32 s96, s96, %0\n\ts_cmp_lt_i32 s96, 0\n\ts_cbranch_scc1 1b"
               :: "s"(due) : "s96", "s97", "scc", "memory");
}

__global__ void __launch_bounds__(NWAVES * 64, 3)
fused_kernel(const unsigned short* __restrict__ x, const unsigned short* __restrict__ kc, const unsigned short* __restrict__ vc,
             const unsigned short* __restrict__ wq, const unsigned short* __restrict__ wo, unsigned short* __restrict__ out,
             unsigned char* __restrict__ ws) {
  __shared__ __attribute__((aligned(16))) unsigned char smem[L_BYTES];
  LAS unsigned char* L = (LAS unsigned char*)smem;
  const int bid = blockIdx.x;
  const int kvh = (bid & 7) * 2 + ((bid >> 3) & 1), b = bid >> 4;
  const int tid = threadIdx.x, l = tid & 63, w = __builtin_amdgcn_readfirstlane(tid >> 6), g = l >> 4, c = l & 15;
  const int sw = w - 1;
  unsigned pf;
  {
    unsigned long long pc; unsigned clen;
    asm volatile("s_getpc_b64 %0\n.Lfused_code_begin:\n\ts_mov_b32 %1, .Lfused_code_end-.Lfused_code_begin" : "=s"(pc), "=s"(clen));
    pf = __builtin_amdgcn_raw_buffer_load_b32(make_rsrc((const void*)pc, (int)clen), tid < 8 ? (tid * 32 + (bid >> 3)) * 128 : 0x7fff0000, 0, 0);
  }
  *(LAS unsigned*)(L + L_FLAG + (l & 15) * 4) = 0u;
  const bool oproj = b < 9;
  const int offa_s = ((b * HKV + kvh) * SK + (SK - W) - 4) * (DH * 2) + tid * 16, offa_q = (l >> 1) * (NQ * 2) + (l & 1) * 16 + (kvh * 256 + b * 16) * 2;
  const int offb_s = (kvh * 256 - 32 + (tid >> 6) * 32 + (l >> 3)) * (D * 2) + (oproj ? b * 128 : 0x7f000000) + (l & 7) * 16, offb_q = c * (D * 2) + g * 16;
  int offa = tid < 64 ? offa_q : offa_s, offb = tid < 64 ? offb_q : offb_s;
  const int rot8k = (bid >> 3) * 8192;
  asm volatile("" : "+v"(offa), "+v"(offb));
  SB();
  asm volatile("" ::"s"(x), "s"(kc), "s"(vc), "s"(wq), "s"(wo), "s"(out), "s"(ws));
  const unsigned t0 = (unsigned)__builtin_amdgcn_s_memrealtime();
  SB();
  const __amdgpu_buffer_rsrc_t rws = make_rsrc(ws, WS_END);
  const int l16 = l * 16;
  unsigned long long e1;
  unsigned ta, tb;
#define EPOCH_GET() do { asm volatile("s_load_dwordx2 %0, %1, 0x0\n\ts_waitcnt lgkmcnt(0)" : "=s"(e1) : "s"(ws + WS_EPOCH) : "memory"); SB(); \
    e1 += 1ull; ta = (unsigned)e1; tb = (unsigned)(e1 >> 32) ^ (ta * 0x9E3779B1u); } while (0)

  if (w == 0) {
    __builtin_amdgcn_s_setprio(3);
    {
      const __amdgpu_buffer_rsrc_t rwq = make_rsrc(wq, D * NQ * 2);
      const __amdgpu_buffer_rsrc_t rx = make_rsrc(x, B * D * 2);
      u32x4 wr[18], xr[18];
#pragma unroll
      for (int s = 0; s < 18; ++s) {
        wr[s] = __builtin_amdgcn_raw_buffer_load_b128(rwq, offa, s * 32 * NQ * 2, AUX_NT); SB();
        xr[s] = __builtin_amdgcn_raw_buffer_load_b128(rx, offb + s * 64, 0, 0); SB();
        if (s == 2) BARRIER();
      }
      EPOCH_GET();
      if (bid < 8 && l < 8) st_unit(rws, WS_TBOX + bid * 128 + l16, u32x4{t0, ta, t0, tb});
#pragma unroll
      for (int s = 0; s < 18; ++s) *(LAS u32x4*)(L + L_WQ + s * 1024 + l16) = wr[s];
      f32x4 acc = {0.f, 0.f, 0.f, 0.f};
      const int troff = (8 * g + (c >> 2)) * 32 + (c & 3) * 8;
#pragma unroll
      for (int s = 0; s < 18; ++s) acc = mfma16(tr_frag(L + L_WQ + s * 1024 + troff, 128), xr[s], acc);
      st_unit(rws, WS_QBOX + kvh * 16384 + c * 1024 + (b * 4 + g) * 16, u32x4{pk_bf16(acc[0], acc[1]), ta, pk_bf16(acc[2], acc[3]), tb});
    }
    asm volatile("" ::"v"(pf));
    while ((unsigned)__builtin_amdgcn_s_memrealtime() - t0 < (unsigned)P_TPOLL) __builtin_amdgcn_s_sleep(4);
    {
      const u32x4 tu = ld_unit(rws, (l & 7) * 128, WS_TBOX);
      u32x4 qa = ld_unit(rws, l16, WS_QBOX + (kvh * 16 + b) * 1024);
      int d = bad_unit(tu, ta, tb) == 0u ? (int)(tu.x - t0) : 0;
      d = min(max(d, -300), 300);
      d += __builtin_amdgcn_mov_dpp(d, XOR1, 0xf, 0xf, true); d += __builtin_amdgcn_mov_dpp(d, XOR2, 0xf, 0xf, true);
      d += __builtin_amdgcn_mov_dpp(d, XOR7, 0xf, 0xf, true); d >>= 3;
      *(LAS unsigned*)(L + L_TREF) = t0 + (unsigned)d;
      for (unsigned spins = 0; !__all(bad_unit(qa, ta, tb) == 0u) && spins < SPIN_CAP; ++spins) {
        __builtin_amdgcn_s_sleep(8);
        CFENCE();
        qa = ld_unit(rws, l16, WS_QBOX + (kvh * 16 + b) * 1024);
      }
      *(LAS u32x2*)(L + L_Q + l * 8) = u32x2{qa.x, qa.z};
      asm volatile("s_waitcnt lgkmcnt(0)" ::: "memory");
      *(volatile LAS unsigned*)(L + L_FLAG) = 1u;
    }
    __builtin_amdgcn_s_setprio(0);
    for (unsigned spins = 0; !__all(*(volatile LAS unsigned*)(L + L_MLF + (l & 7) * 4) != 0u) && spins < (SPIN_CAP << 4); ++spins) __builtin_amdgcn_s_sleep(16);
    CFENCE();
    float fw[8];
    {
      const int h = l >> 5;
      float mw[8], lw[8];
#pragma unroll
      for (int ww = 0; ww < 8; ++ww) { mw[ww] = *(LAS float*)(L + L_ML + ww * 16 + h * 4); lw[ww] = *(LAS float*)(L + L_ML + ww * 16 + 8 + h * 4); }
      float M = mw[0];
#pragma unroll
      for (int ww = 1; ww < 8; ++ww) M = fmaxf(M, mw[ww]);
      float Ls = 0.f;
#pragma unroll
      for (int ww = 0; ww < 8; ++ww) { fw[ww] = __builtin_amdgcn_exp2f(mw[ww] - M); Ls = fmaf(fw[ww], lw[ww], Ls); }
      const float inv = 1.0f / Ls;
#pragma unroll
      for (int ww = 0; ww < 8; ++ww) { fw[ww] *= inv; asm volatile("" : "+v"(fw[ww])); }
    }
    int ooff = WS_OBOX + (kvh * 16 + b) * 1024 + l16;
    asm volatile("" : "+v"(ooff));
    __builtin_amdgcn_s_setprio(3);
    BARRIER();
    f32x4 ow[8];
#pragma unroll
    for (int ww = 0; ww < 8; ++ww) ow[ww] = *(LAS f32x4*)(L + L_RED + ww * 1024 + l16);
    const f32x4 O = ((fw[0] * ow[0] + fw[1] * ow[1]) + (fw[2] * ow[2] + fw[3] * ow[3])) + ((fw[4] * ow[4] + fw[5] * ow[5]) + (fw[6] * ow[6] + fw[7] * ow[7]));
    st_unit(rws, ooff, u32x4{pk_bf16(O.x, O.y), ta, pk_bf16(O.z, O.w), tb});
    if (oproj) BARRIER();
  } else {
    const __amdgpu_buffer_rsrc_t rk = make_rsrc(kc, B * HKV * SK * DH * 2);
    const __amdgpu_buffer_rsrc_t rv = make_rsrc(vc, B * HKV * SK * DH * 2);
    unsigned tp0 = 0;
    u32x4 R[64];
    const __amdgpu_buffer_rsrc_t rwo = make_rsrc(wo, NQ * D * 2);
    u32x4 wor[4];
    constexpr auto item = [](int j) constexpr { return j == 0 ? 0 : (j == 1 ? 32 : (j <= 32 ? j - 1 : j)); };
    constexpr auto pos = [](int i) constexpr { return i == 0 ? 0 : (i == 32 ? 1 : (i < 32 ? i + 1 : i)); };
#define ISSUE(j) do { constexpr int it_ = item(j);                                                                               \
      if constexpr ((j) >= W0) { wait_vm<WIN - 1>(); pace_until(tp0 + (unsigned)due(j)); SB(); }                                 \
      R[it_] = __builtin_amdgcn_raw_buffer_load_b128(it_ < 32 ? rk : rv, offa, (it_ * 8192 + rot8k) & 0x3ffff, AUX_NT); SB(); } while (0)
    auto phase_a = [&](auto self, auto jc) {
      constexpr int j = decltype(jc)::value;
      if constexpr (j < KB) {
        ISSUE(j);
        if constexpr (j == 1) {
#pragma unroll
          for (int u = 0; u < 4; ++u) { wor[u] = __builtin_amdgcn_raw_buffer_load_b128(rwo, offb, u * 8 * D * 2, AUX_NT); SB(); }
        }
        if constexpr (j == W0 - 1) {
          BARRIER(); EPOCH_GET();
          tp0 = t0; asm volatile("" : "+s"(tp0)); tp0 += (unsigned)(P_PACE0 + ((sw * P_PACE) >> 5));
        }
        if constexpr (j == W0) {
#pragma unroll
          for (int u = 0; u < 4; ++u) *(LAS u32x4*)(L + L_WO + (sw * 32 + u * 8 + (l >> 3)) * ROWP + (l & 7) * 16) = wor[u];
          SB();
        }
        self(self, std::integral_constant<int, j + 1>{});
      }
    };
    phase_a(phase_a, std::integral_constant<int, 0>{});
    asm volatile("" ::"v"(pf));
    for (unsigned spins = 0; *(volatile LAS unsigned*)(L + L_FLAG) == 0u && spins < (SPIN_CAP << 4); ++spins) __builtin_amdgcn_s_sleep(1);
    CFENCE();
    {
      const int dt = (int)((unsigned)__builtin_amdgcn_readfirstlane(*(LAS unsigned*)(L + L_TREF)) - t0);
      tp0 = t0 + (unsigned)(min(max(dt, -400), 400) + P_PACE1 + ((sw * P_PACE) >> 5));
    }
    const u32x4 q0 = *(LAS u32x4*)(L + L_Q + c * 16), q1 = *(LAS u32x4*)(L + L_Q + 256 + c * 16);
    const bool up8 = (c & 8) != 0, up4 = (c & 4) != 0, up2 = (c & 2) != 0, up1 = (c & 1) != 0;
    float u0[4], u1[4], sc[2][2];
    const int hh = c >> 3, jj = c & 7;
    const unsigned pick = (unsigned)((2 * hh + 1) << 8 | (2 * hh));
    const unsigned act = (jj & 1) ? (pick << 16 | 0x0C0Cu) : (0x0C0C0000u | pick);
    u32x4 sel;
    sel.x = (jj >> 1) == 0 ? act : 0x0C0C0C0Cu; sel.y = (jj >> 1) == 1 ? act : 0x0C0C0C0Cu;
    sel.z = (jj >> 1) == 2 ? act : 0x0C0C0C0Cu; sel.w = (jj >> 1) == 3 ? act : 0x0C0C0C0Cu;
    unsigned P0 = 0, P1 = 0;
    f32x4 acc = {0.f, 0.f, 0.f, 0.f};
    u32x4 afr = {0u, 0u, 0u, 0u};
    constexpr float C2 = 0.088388347648318440f * 1.4426950408889634f;
#define COMB(UP, CTRL, LO, HI) ((UP ? HI : LO) + dpp<CTRL>(UP ? LO : HI))
#define AFRAG(I) do { const unsigned t_ = dppu<BCAST + bitrev4((I) & 15)>((I) < 16 ? P0 : P1);                                   \
      afr.x = __builtin_amdgcn_perm(t_, t_, sel.x); afr.y = __builtin_amdgcn_perm(t_, t_, sel.y);                                \
      afr.z = __builtin_amdgcn_perm(t_, t_, sel.z); afr.w = __builtin_amdgcn_perm(t_, t_, sel.w); } while (0)
    auto process = [&](auto ic) {
      constexpr int i = decltype(ic)::value;
      if constexpr (i < 32) {
        const u32x4 kk = R[i];
        float a = dot2(kk.x, q0.x, 0.f), bb = dot2(kk.x, q1.x, 0.f);
        a = dot2(kk.y, q0.y, a); bb = dot2(kk.y, q1.y, bb);
        a = dot2(kk.z, q0.z, a); bb = dot2(kk.z, q1.z, bb);
        a = dot2(kk.w, q0.w, a); bb = dot2(kk.w, q1.w, bb);
        if constexpr (!(i & 1)) { u0[0] = a; u1[0] = bb; }
        else {
          a = COMB(up8, XOR8, u0[0], a); bb = COMB(up8, XOR8, u1[0], bb);
          if constexpr (!(i & 2)) { u0[1] = a; u1[1] = bb; }
          else {
            a = COMB(up4, XOR7, u0[1], a); bb = COMB(up4, XOR7, u1[1], bb);
            if constexpr (!(i & 4)) { u0[2] = a; u1[2] = bb; }
            else {
              a = COMB(up2, XOR2, u0[2], a); bb = COMB(up2, XOR2, u1[2], bb);
              if constexpr (!(i & 8)) { u0[3] = a; u1[3] = bb; }
              else {
                a = COMB(up1, XOR1, u0[3], a); bb = COMB(up1, XOR1, u1[3], bb);
                sc[i >> 4][0] = a; sc[i >> 4][1] = bb;
              }
            }
          }
        }
        if constexpr (i == 31) {
          float m[2], lsum[2], p[2][2];
#pragma unroll
          for (int h = 0; h < 2; ++h) {
            const float s0 = sc[0][h] * C2, s1 = sc[1][h] * C2;
            m[h] = wave_max(fmaxf(s0, s1));
            p[0][h] = __builtin_amdgcn_exp2f(s0 - m[h]); p[1][h] = __builtin_amdgcn_exp2f(s1 - m[h]);
            lsum[h] = wave_sum(p[0][h] + p[1][h]);
          }
          P0 = pk_bf16(p[0][0], p[0][1]); P1 = pk_bf16(p[1][0], p[1][1]);
          if (l == 0) {
            *(LAS f32x4*)(L + L_ML + sw * 16) = f32x4{m[0], m[1], lsum[0], lsum[1]};
            CFENCE();
            *(volatile LAS unsigned*)(L + L_MLF + sw * 4) = 1u;
          }
          AFRAG(0);
        }
      } else {
        acc = mfma16(afr, R[i], acc);
        if constexpr (i < 63) AFRAG(i - 31);
      }
      SB();
    };
    auto phase_b = [&](auto self, auto jc, auto dc) {
      constexpr int j = decltype(jc)::value, done = decltype(dc)::value;
      if constexpr (done < 64) {
        constexpr int landed = j < 64 ? j - WIN : 64;
        auto run = [&](auto self2, auto ic) {
          constexpr int i = decltype(ic)::value;
          if constexpr (i < 64 && i < done + CHUNK && pos(i < 64 ? i : 0) < landed) { process(ic); return self2(self2, std::integral_constant<int, i + 1>{}); }
          else return ic;
        };
        auto nd = run(run, std::integral_constant<int, done>{});
        if constexpr (j < 64) ISSUE(j);
        self(self, std::integral_constant<int, (j < 64 ? j + 1 : 64)>{}, nd);
      }
    };
    phase_b(phase_b, std::integral_constant<int, KB>{}, std::integral_constant<int, 0>{});
#undef COMB
#undef AFRAG
#undef ISSUE
    *(LAS f32x4*)(L + L_RED + sw * 1024 + (g >> 1) * 512 + c * 32 + (g & 1) * 16) = acc;
    BARRIER();
    if (oproj) {
      const int off = (c & 7) * 1024 + sw * 128 + g * 32 + (c >> 3) * 16;
      __builtin_amdgcn_s_sleep(P_D1);
      SB();
      u32x4 a0 = ld_unit(rws, off, WS_OBOX + kvh * 16384), a1 = ld_unit(rws, off, WS_OBOX + kvh * 16384 + 8192);
      SB();
      u32x4 wfr[4];
#pragma unroll
      for (int t = 0; t < 4; ++t) wfr[t] = tr_frag(L + L_WO + (sw * 32 + 8 * g + (c >> 2)) * ROWP + t * 32 + (c & 3) * 8, 4 * ROWP);
      SB();
      {
        bool ok = (bad_unit(a0, ta, tb) | bad_unit(a1, ta, tb)) == 0u;
        for (unsigned spins = 0; !__all(ok) && spins < SPIN_CAP; ++spins) {
          CFENCE();
          if (!ok) {
            a0 = ld_unit(rws, off, WS_OBOX + kvh * 16384);
            a1 = ld_unit(rws, off, WS_OBOX + kvh * 16384 + 8192);
            ok = (bad_unit(a0, ta, tb) | bad_unit(a1, ta, tb)) == 0u;
          }
        }
      }
      const bool lo8 = c < 8;
      const unsigned gx = dppu<XOR8>(lo8 ? a1.x : a0.x), gz = dppu<XOR8>(lo8 ? a1.z : a0.z);
      const u32x4 of = {lo8 ? a0.x : gx, lo8 ? a0.z : gz, lo8 ? gx : a1.x, lo8 ? gz : a1.z};
#pragma unroll
      for (int t = 0; t < 4; ++t) {
        const f32x4 d = mfma16(wfr[t], of, f32x4{0.f, 0.f, 0.f, 0.f});
        *(LAS f32x4*)(L + L_RED2 + (sw * 4 + t) * 1024 + l16) = d;
      }
      BARRIER();
      if (sw < 4) {
        f32x4 s = *(LAS f32x4*)(L + L_RED2 + sw * 1024 + l16);
#pragma unroll
        for (int ww = 1; ww < 8; ++ww) s += *(LAS f32x4*)(L + L_RED2 + (ww * 4 + sw) * 1024 + l16);
        const bool lo = c < 8;
        const unsigned v0 = __float_as_uint(s.x), v1 = __float_as_uint(s.y), n0 = dppu<XOR8>(__float_as_uint(s.z)), n1 = dppu<XOR8>(__float_as_uint(s.w));
        const int base = WS_PBOX + (((c & 7) * 16 + kvh) * 288 + b * 32 + sw * 8 + g * 2) * 16;
        st_unit(rws, base + (lo ? 0 : 16), u32x4{lo ? v0 : n0, ta, lo ? v1 : n1, tb});
        st_unit(rws, base + 8 * 16 * 288 * 16 + (lo ? 16 : 0), u32x4{lo ? n0 : v0, ta, lo ? n1 : v1, tb});
      } else {
        const int poff = ((kvh * 16 + (l >> 3)) * 288 + b * 32 + (sw - 4) * 8 + (l & 7)) * 16;
        __builtin_amdgcn_s_sleep(P_D2);
        u32x4 p0 = ld_unit(rws, poff, WS_PBOX), p1 = ld_unit(rws, poff + 8 * 288 * 16, WS_PBOX);
        bool ok0 = bad_unit(p0, ta, tb) == 0u, ok1 = bad_unit(p1, ta, tb) == 0u;
        for (unsigned spins = 0; !__all(ok0 && ok1) && spins < SPIN_CAP; ++spins) {
          CFENCE();
          if (!ok0) p0 = ld_unit(rws, poff, WS_PBOX);
          if (!ok1) p1 = ld_unit(rws, poff + 8 * 288 * 16, WS_PBOX);
          ok0 = bad_unit(p0, ta, tb) == 0u; ok1 = bad_unit(p1, ta, tb) == 0u;
        }
        float v0 = __uint_as_float(p0.x) + __uint_as_float(p1.x), v1 = __uint_as_float(p0.z) + __uint_as_float(p1.z);
        v0 += dpp<XOR8>(v0); v1 += dpp<XOR8>(v1);
        v0 = xrow_sum(v0); v1 = xrow_sum(v1);
        if (l < 8) *(unsigned*)(out + kvh * D + b * 64 + (sw - 4) * 16 + l * 2) = pk_bf16(v0, v1);
        if (bid == 0 && tid == 5 * 64) __builtin_amdgcn_raw_buffer_store_b64(u32x2{ta, (unsigned)(e1 >> 32)}, rws, 0, WS_EPOCH, AUX_SC1);
      }
    }
  }
  asm volatile(".Lfused_code_end:");
}

extern "C" void kernel_launch(void* const* d_in, const int* in_sizes, int n_in, void* d_out, int out_size, void* d_ws,
                              size_t ws_size, hipStream_t stream) {
  fused_kernel<<<256, NWAVES * 64, 0, stream>>>((const unsigned short*)d_in[0], (const unsigned short*)d_in[1], (const unsigned short*)d_in[2],
                                                (const unsigned short*)d_in[3], (const unsigned short*)d_in[4], (unsigned short*)d_out,
                                                (unsigned char*)d_ws);
}
```

```cpp
#include <hip/hip_runtime.h>
#include <hip/hip_bf16.h>
#include <cstdint>
#include <type_traits>

#define DIAG 0
#define P_PACE 118
#define P_PACE0 185
#define P_PACE1 170
#define P_TPOLL 660
#define P_D1 14
#define P_D2 25

#define P_J1 64
#define P_PACEB 120
constexpr int due(int j) { return (j <= P_J1 ? j * P_PACE : P_J1 * P_PACE + (j - P_J1) * P_PACEB) >> 2; }
constexpr int B = 16, D = 576, HQ = 32, HKV = 16, SK = 4096, DH = 128, W = 1024;
constexpr int NQ = HQ * DH;
constexpr int NWAVES = 9;
constexpr int W0 = 4;
constexpr int WIN = 6;
constexpr int KB = 30;
constexpr int CHUNK = 3;

typedef unsigned u32x4 __attribute__((ext_vector_type(4)));
typedef unsigned u32x2 __attribute__((ext_vector_type(2)));
typedef float f32x4 __attribute__((ext_vector_type(4)));
typedef short bf16x8 __attribute__((ext_vector_type(8)));
typedef short s16x4 __attribute__((ext_vector_type(4)));
typedef __bf16 bf16x2 __attribute__((ext_vector_type(2)));
#define LAS __attribute__((address_space(3)))

constexpr int AUX_SC1 = 16, AUX_NT = 2;
constexpr int RSRC_W3 = 0x00020000;

constexpr int WS_EPOCH = 0;
constexpr int WS_TBOX = 1024;
constexpr int WS_QBOX = 4096;
constexpr int WS_OBOX = WS_QBOX + 16 * 16 * 1024;
constexpr int WS_PBOX = WS_OBOX + 16 * 16 * 1024;
constexpr int WS_END = WS_PBOX + 16 * 16 * 288 * 16;
constexpr int WS_DIAG = 8 << 20;
constexpr unsigned SPIN_CAP = 1u << 18;
constexpr int bitrev4(int i) { return ((i & 1) << 3) | ((i & 2) << 1) | ((i & 4) >> 1) | ((i & 8) >> 3); }

constexpr int ROWP = 144;
constexpr int L_RED = 0;
constexpr int L_ML = 8192;
constexpr int L_Q = L_ML + 128;
constexpr int L_FLAG = L_Q + 512;
constexpr int L_TREF = L_FLAG + 4;
constexpr int L_MLF = L_FLAG + 16;
constexpr int L_RED2 = 9216;
constexpr int L_WQ = L_RED2;
constexpr int L_WO = L_RED2 + 32768;
constexpr int L_BYTES = L_WO + 256 * ROWP;

__device__ __forceinline__ float dot2(unsigned k, unsigned q, float acc) {
  return __builtin_amdgcn_fdot2_f32_bf16(__builtin_bit_cast(bf16x2, k), __builtin_bit_cast(bf16x2, q), acc, false);
}
__device__ __forceinline__ unsigned pk_bf16(float lo, float hi) {
  bf16x2 v; v.x = (__bf16)lo; v.y = (__bf16)hi;
  return __builtin_bit_cast(unsigned, v);
}
template <int CTRL> __device__ __forceinline__ float dpp(float x) {
  return __builtin_bit_cast(float, __builtin_amdgcn_mov_dpp(__builtin_bit_cast(int, x), CTRL, 0xf, 0xf, true));
}
template <int CTRL> __device__ __forceinline__ unsigned dppu(unsigned x) {
  return (unsigned)__builtin_amdgcn_mov_dpp((int)x, CTRL, 0xf, 0xf, true);
}
constexpr int XOR1 = 0xB1, XOR2 = 0x4E, XOR7 = 0x141, XOR8 = 0x128, BCAST = 0x150;
__device__ __forceinline__ float xrow_max(float x) {
  auto s = __builtin_amdgcn_permlane16_swap(__float_as_uint(x), __float_as_uint(x), false, false);
  x = fmaxf(__uint_as_float(s[0]), __uint_as_float(s[1]));
  auto t = __builtin_amdgcn_permlane32_swap(__float_as_uint(x), __float_as_uint(x), false, false);
  return fmaxf(__uint_as_float(t[0]), __uint_as_float(t[1]));
}
__device__ __forceinline__ float xrow_sum(float x) {
  auto s = __builtin_amdgcn_permlane16_swap(__float_as_uint(x), __float_as_uint(x), false, false);
  x = __uint_as_float(s[0]) + __uint_as_float(s[1]);
  auto t = __builtin_amdgcn_permlane32_swap(__float_as_uint(x), __float_as_uint(x), false, false);
  return __uint_as_float(t[0]) + __uint_as_float(t[1]);
}
__device__ __forceinline__ float wave_max(float x) {
  x = fmaxf(x, dpp<XOR1>(x)); x = fmaxf(x, dpp<XOR2>(x)); x = fmaxf(x, dpp<XOR7>(x)); x = fmaxf(x, dpp<XOR8>(x));
  return xrow_max(x);
}
__device__ __forceinline__ float wave_sum(float x) {
  x += dpp<XOR1>(x); x += dpp<XOR2>(x); x += dpp<XOR7>(x); x += dpp<XOR8>(x);
  return xrow_sum(x);
}
__device__ __forceinline__ f32x4 mfma16(u32x4 a, u32x4 b, f32x4 c) {
  return __builtin_amdgcn_mfma_f32_16x16x32_bf16(__builtin_bit_cast(bf16x8, a), __builtin_bit_cast(bf16x8, b), c, 0, 0, 0);
}
__device__ __forceinline__ __amdgpu_buffer_rsrc_t make_rsrc(const void* p, int bytes) {
  return __builtin_amdgcn_make_buffer_rsrc((void*)p, 0, bytes, RSRC_W3);
}
__device__ __forceinline__ u32x4 tr_frag(LAS unsigned char* p, int second) {
  s16x4 lo = __builtin_amdgcn_ds_read_tr16_b64_v4i16((LAS s16x4*)p);
  s16x4 hi = __builtin_amdgcn_ds_read_tr16_b64_v4i16((LAS s16x4*)(p + second));
  u32x2 a = __builtin_bit_cast(u32x2, lo), b = __builtin_bit_cast(u32x2, hi);
  return u32x4{a.x, a.y, b.x, b.y};
}
__device__ __forceinline__ unsigned bad_unit(u32x4 v, unsigned ta, unsigned tb) { return (v.y ^ ta) | (v.w ^ tb); }
__device__ __forceinline__ void st_unit(__amdgpu_buffer_rsrc_t r, int off, u32x4 v) { __builtin_amdgcn_raw_buffer_store_b128(v, r, off, 0, AUX_SC1); }
__device__ __forceinline__ u32x4 ld_unit(__amdgpu_buffer_rsrc_t r, int off, int soff) { return __builtin_amdgcn_raw_buffer_load_b128(r, off, soff, AUX_SC1); }
#define SB() __builtin_amdgcn_sched_barrier(0)
#define CFENCE() asm volatile("" ::: "memory")
#define BARRIER() do { CFENCE(); asm volatile("s_waitcnt lgkmcnt(0)" ::: "memory"); __builtin_amdgcn_s_barrier(); CFENCE(); SB(); } while (0)
template <int N> __device__ __forceinline__ void wait_vm() { asm volatile("s_waitcnt vmcnt(%0)" ::"n"(N) : "memory"); }
__device__ __forceinline__ void pace_until(unsigned due) {
  asm volatile("1:\n\ts_memrealtime s[96:97]\n\ts_waitcnt lgkmcnt(0)\n\ts_sub_u32 s96, s96, %0\n\ts_cmp_lt_i32 s96, 0\n\ts_cbranch_scc1 1b"
               :: "s"(due) : "s96", "s97", "scc", "memory");
}

#define STAMP(i) do { } while (0)

__global__ void __launch_bounds__(NWAVES * 64, 3)
fused_kernel(const unsigned short* __restrict__ x, const unsigned short* __restrict__ kc, const unsigned short* __restrict__ vc,
             const unsigned short* __restrict__ wq, const unsigned short* __restrict__ wo, unsigned short* __restrict__ out,
             unsigned char* __restrict__ ws) {
  __shared__ __attribute__((aligned(16))) unsigned char smem[L_BYTES];
  LAS unsigned char* L = (LAS unsigned char*)smem;
  const int bid = blockIdx.x;
  const int kvh = (bid & 7) * 2 + ((bid >> 3) & 1), b = bid >> 4;
  const int tid = threadIdx.x, l = tid & 63, w = __builtin_amdgcn_readfirstlane(tid >> 6), g = l >> 4, c = l & 15;
  const int sw = w - 1;
  unsigned long long t1_, t2_, t3_, kd_;
  asm volatile("s_memrealtime %0\n\ts_waitcnt lgkmcnt(0)" : "=s"(t1_)::"memory");
  asm volatile("s_load_dwordx2 %0, %1, 0x0\n\ts_waitcnt lgkmcnt(0)" : "=s"(kd_) : "s"(__builtin_amdgcn_kernarg_segment_ptr()) : "memory");
  asm volatile("s_memrealtime %0\n\ts_waitcnt lgkmcnt(0)" : "=s"(t2_)::"memory"); SB();
  unsigned pf;
  {
    unsigned long long pc; unsigned clen;
    asm volatile("s_getpc_b64 %0\n.Lfused_code_begin:\n\ts_mov_b32 %1, .Lfused_code_end-.Lfused_code_begin" : "=s"(pc), "=s"(clen));
    pf = __builtin_amdgcn_raw_buffer_load_b32(make_rsrc((const void*)pc, (int)clen), tid < 8 ? (tid * 32 + (bid >> 3)) * 128 : 0x7fff0000, 0, 0);
  }
  unsigned long long t64 = __builtin_amdgcn_s_memrealtime();
  *(LAS unsigned*)(L + L_FLAG + (l & 15) * 4) = 0u;
  const bool oproj = b < 9;
  const int offa_s = ((b * HKV + kvh) * SK + (SK - W) - 4) * (DH * 2) + tid * 16, offa_q = (l >> 1) * (NQ * 2) + (l & 1) * 16 + (kvh * 256 + b * 16) * 2;
  const int offb_s = (kvh * 256 - 32 + (tid >> 6) * 32 + (l >> 3)) * (D * 2) + (oproj ? b * 128 : 0x7f000000) + (l & 7) * 16, offb_q = c * (D * 2) + g * 16;
  int offa = tid < 64 ? offa_q : offa_s, offb = tid < 64 ? offb_q : offb_s;
  const int rot8k = (bid >> 3) * 8192;
  asm volatile("" : "+v"(offa), "+v"(offb));
  SB();
  asm volatile("" : "+s"(t64) : "s"(x), "s"(kc), "s"(vc), "s"(wq), "s"(wo), "s"(out), "s"(ws));
  const unsigned t0 = (unsigned)t64;
  asm volatile("s_memrealtime %0\n\ts_waitcnt lgkmcnt(0)" : "=s"(t3_)::"memory"); SB();
  const __amdgpu_buffer_rsrc_t rws = make_rsrc(ws, WS_END);
  const int l16 = l * 16;
  unsigned long long e1;
  unsigned ta, tb;
#define EPOCH_GET() do { asm volatile("s_load_dwordx2 %0, %1, 0x0\n\ts_waitcnt lgkmcnt(0)" : "=s"(e1) : "s"(ws + WS_EPOCH) : "memory"); SB(); \
    e1 += 1ull; ta = (unsigned)e1; tb = (unsigned)(e1 >> 32) ^ (ta * 0x9E3779B1u); } while (0)

  if (w == 0) {
    __builtin_amdgcn_s_setprio(3);
    {
      const __amdgpu_buffer_rsrc_t rwq = make_rsrc(wq, D * NQ * 2);
      const __amdgpu_buffer_rsrc_t rx = make_rsrc(x, B * D * 2);
      u32x4 wr[18], xr[18];
#pragma unroll
      for (int s = 0; s < 18; ++s) {
        wr[s] = __builtin_amdgcn_raw_buffer_load_b128(rwq, offa, s * 32 * NQ * 2, AUX_NT); SB();
        xr[s] = __builtin_amdgcn_raw_buffer_load_b128(rx, offb + s * 64, 0, 0); SB();
        if (s == 2) BARRIER();
      }
      EPOCH_GET();
      if (bid < 8 && l < 8) st_unit(rws, WS_TBOX + bid * 128 + l16, u32x4{t0, ta, t0, tb});
      STAMP(1);
#pragma unroll
      for (int s = 0; s < 18; ++s) *(LAS u32x4*)(L + L_WQ + s * 1024 + l16) = wr[s];
      f32x4 acc = {0.f, 0.f, 0.f, 0.f};
      const int troff = (8 * g + (c >> 2)) * 32 + (c & 3) * 8;
#pragma unroll
      for (int s = 0; s < 18; ++s) acc = mfma16(tr_frag(L + L_WQ + s * 1024 + troff, 128), xr[s], acc);
      STAMP(2);
      st_unit(rws, WS_QBOX + kvh * 16384 + c * 1024 + (b * 4 + g) * 16, u32x4{pk_bf16(acc[0], acc[1]), ta, pk_bf16(acc[2], acc[3]), tb});
      STAMP(3);
    }
    asm volatile("" ::"v"(pf));
    while ((unsigned)__builtin_amdgcn_s_memrealtime() - t0 < (unsigned)P_TPOLL) __builtin_amdgcn_s_sleep(4);
    {
      const u32x4 tu = ld_unit(rws, (l & 7) * 128, WS_TBOX);
      u32x4 qa = ld_unit(rws, l16, WS_QBOX + (kvh * 16 + b) * 1024);
      int d = bad_unit(tu, ta, tb) == 0u ? (int)(tu.x - t0) : 0;
      d = min(max(d, -300), 300);
      d += __builtin_amdgcn_mov_dpp(d, XOR1, 0xf, 0xf, true); d += __builtin_amdgcn_mov_dpp(d, XOR2, 0xf, 0xf, true);
      d += __builtin_amdgcn_mov_dpp(d, XOR7, 0xf, 0xf, true); d >>= 3;
      *(LAS unsigned*)(L + L_TREF) = t0 + (unsigned)d;
      for (unsigned spins = 0; !__all(bad_unit(qa, ta, tb) == 0u) && spins < SPIN_CAP; ++spins) {
        __builtin_amdgcn_s_sleep(8);
        CFENCE();
        qa = ld_unit(rws, l16, WS_QBOX + (kvh * 16 + b) * 1024);
      }
      *(LAS u32x2*)(L + L_Q + l * 8) = u32x2{qa.x, qa.z};
      asm volatile("s_waitcnt lgkmcnt(0)" ::: "memory");
      *(volatile LAS unsigned*)(L + L_FLAG) = 1u;
    }
    STAMP(4);
    __builtin_amdgcn_s_setprio(0);
    for (unsigned spins = 0; !__all(*(volatile LAS unsigned*)(L + L_MLF + (l & 7) * 4) != 0u) && spins < (SPIN_CAP << 4); ++spins) __builtin_amdgcn_s_sleep(16);
    CFENCE();
    float fw[8];
    {
      const int h = l >> 5;
      float mw[8], lw[8];
#pragma unroll
      for (int ww = 0; ww < 8; ++ww) { mw[ww] = *(LAS float*)(L + L_ML + ww * 16 + h * 4); lw[ww] = *(LAS float*)(L + L_ML + ww * 16 + 8 + h * 4); }
      float M = mw[0];
#pragma unroll
      for (int ww = 1; ww < 8; ++ww) M = fmaxf(M, mw[ww]);
      float Ls = 0.f;
#pragma unroll
      for (int ww = 0; ww < 8; ++ww) { fw[ww] = __builtin_amdgcn_exp2f(mw[ww] - M); Ls = fmaf(fw[ww], lw[ww], Ls); }
      const float inv = 1.0f / Ls;
#pragma unroll
      for (int ww = 0; ww < 8; ++ww) { fw[ww] *= inv; asm volatile("" : "+v"(fw[ww])); }
    }
    int ooff = WS_OBOX + (kvh * 16 + b) * 1024 + l16;
    asm volatile("" : "+v"(ooff));
    __builtin_amdgcn_s_setprio(3);
    BARRIER();
    STAMP(6);
    f32x4 ow[8];
#pragma unroll
    for (int ww = 0; ww < 8; ++ww) ow[ww] = *(LAS f32x4*)(L + L_RED + ww * 1024 + l16);
    const f32x4 O = ((fw[0] * ow[0] + fw[1] * ow[1]) + (fw[2] * ow[2] + fw[3] * ow[3])) + ((fw[4] * ow[4] + fw[5] * ow[5]) + (fw[6] * ow[6] + fw[7] * ow[7]));
    st_unit(rws, ooff, u32x4{pk_bf16(O.x, O.y), ta, pk_bf16(O.z, O.w), tb});
    STAMP(7);
    if (oproj) BARRIER();
  } else {
    const __amdgpu_buffer_rsrc_t rk = make_rsrc(kc, B * HKV * SK * DH * 2);
    const __amdgpu_buffer_rsrc_t rv = make_rsrc(vc, B * HKV * SK * DH * 2);
    unsigned tp0 = t0 + (unsigned)(P_PACE0 + ((sw * P_PACE) >> 5));
    u32x4 R[64];
    const __amdgpu_buffer_rsrc_t rwo = make_rsrc(wo, NQ * D * 2);
    u32x4 wor[4];
    constexpr auto item = [](int j) constexpr { return j == 0 ? 0 : (j == 1 ? 32 : (j <= 32 ? j - 1 : j)); };
    constexpr auto pos = [](int i) constexpr { return i == 0 ? 0 : (i == 32 ? 1 : (i < 32 ? i + 1 : i)); };
#define ISSUE(j) do { constexpr int it_ = item(j);                                                                               \
      if constexpr ((j) >= W0) { wait_vm<WIN - 1>(); pace_until(tp0 + (unsigned)due(j)); SB(); }                                 \
      R[it_] = __builtin_amdgcn_raw_buffer_load_b128(it_ < 32 ? rk : rv, offa, (it_ * 8192 + rot8k) & 0x3ffff, AUX_NT); SB(); } while (0)
    auto phase_a = [&](auto self, auto jc) {
      constexpr int j = decltype(jc)::value;
      if constexpr (j < KB) {
        ISSUE(j);
        if constexpr (j == 1) {
#pragma unroll
          for (int u = 0; u < 4; ++u) { wor[u] = __builtin_amdgcn_raw_buffer_load_b128(rwo, offb, u * 8 * D * 2, AUX_NT); SB(); }
        }
        if constexpr (j == W0 - 1) { BARRIER(); EPOCH_GET(); }
        if constexpr (j == W0) {
#pragma unroll
          for (int u = 0; u < 4; ++u) *(LAS u32x4*)(L + L_WO + (sw * 32 + u * 8 + (l >> 3)) * ROWP + (l & 7) * 16) = wor[u];
          SB();
        }
        self(self, std::integral_constant<int, j + 1>{});
      }
    };
    phase_a(phase_a, std::integral_constant<int, 0>{});
    STAMP(1);
    asm volatile("" ::"v"(pf));
    for (unsigned spins = 0; *(volatile LAS unsigned*)(L + L_FLAG) == 0u && spins < (SPIN_CAP << 4); ++spins) __builtin_amdgcn_s_sleep(1);
    CFENCE();
    STAMP(4);
    {
      const int dt = (int)((unsigned)__builtin_amdgcn_readfirstlane(*(LAS unsigned*)(L + L_TREF)) - t0);
      tp0 = t0 + (unsigned)(min(max(dt, -400), 400) + P_PACE1 + ((sw * P_PACE) >> 5));
    }
    const u32x4 q0 = *(LAS u32x4*)(L + L_Q + c * 16), q1 = *(LAS u32x4*)(L + L_Q + 256 + c * 16);
    const bool up8 = (c & 8) != 0, up4 = (c & 4) != 0, up2 = (c & 2) != 0, up1 = (c & 1) != 0;
    float u0[4], u1[4], sc[2][2];
    const int hh = c >> 3, jj = c & 7;
    const unsigned pick = (unsigned)((2 * hh + 1) << 8 | (2 * hh));
    const unsigned act = (jj & 1) ? (pick << 16 | 0x0C0Cu) : (0x0C0C0000u | pick);
    u32x4 sel;
    sel.x = (jj >> 1) == 0 ? act : 0x0C0C0C0Cu; sel.y = (jj >> 1) == 1 ? act : 0x0C0C0C0Cu;
    sel.z = (jj >> 1) == 2 ? act : 0x0C0C0C0Cu; sel.w = (jj >> 1) == 3 ? act : 0x0C0C0C0Cu;
    unsigned P0 = 0, P1 = 0;
    f32x4 acc = {0.f, 0.f, 0.f, 0.f};
    u32x4 afr = {0u, 0u, 0u, 0u};
    constexpr float C2 = 0.088388347648318440f * 1.4426950408889634f;
#define COMB(UP, CTRL, LO, HI) ((UP ? HI : LO) + dpp<CTRL>(UP ? LO : HI))
#define AFRAG(I) do { const unsigned t_ = dppu<BCAST + bitrev4((I) & 15)>((I) < 16 ? P0 : P1);                                   \
      afr.x = __builtin_amdgcn_perm(t_, t_, sel.x); afr.y = __builtin_amdgcn_perm(t_, t_, sel.y);                                \
      afr.z = __builtin_amdgcn_perm(t_, t_, sel.z); afr.w = __builtin_amdgcn_perm(t_, t_, sel.w); } while (0)
    auto process = [&](auto ic) {
      constexpr int i = decltype(ic)::value;
      if constexpr (i < 32) {
        const u32x4 kk = R[i];
        float a = dot2(kk.x, q0.x, 0.f), bb = dot2(kk.x, q1.x, 0.f);
        a = dot2(kk.y, q0.y, a); bb = dot2(kk.y, q1.y, bb);
        a = dot2(kk.z, q0.z, a); bb = dot2(kk.z, q1.z, bb);
        a = dot2(kk.w, q0.w, a); bb = dot2(kk.w, q1.w, bb);
        if constexpr (!(i & 1)) { u0[0] = a; u1[0] = bb; }
        else {
          a = COMB(up8, XOR8, u0[0], a); bb = COMB(up8, XOR8, u1[0], bb);
          if constexpr (!(i & 2)) { u0[1] = a; u1[1] = bb; }
          else {
            a = COMB(up4, XOR7, u0[1], a); bb = COMB(up4, XOR7, u1[1], bb);
            if constexpr (!(i & 4)) { u0[2] = a; u1[2] = bb; }
            else {
              a = COMB(up2, XOR2, u0[2], a); bb = COMB(up2, XOR2, u1[2], bb);
              if constexpr (!(i & 8)) { u0[3] = a; u1[3] = bb; }
              else {
                a = COMB(up1, XOR1, u0[3], a); bb = COMB(up1, XOR1, u1[3], bb);
                sc[i >> 4][0] = a; sc[i >> 4][1] = bb;
              }
            }
          }
        }
        if constexpr (i == 31) {
          float m[2], lsum[2], p[2][2];
#pragma unroll
          for (int h = 0; h < 2; ++h) {
            const float s0 = sc[0][h] * C2, s1 = sc[1][h] * C2;
            m[h] = wave_max(fmaxf(s0, s1));
            p[0][h] = __builtin_amdgcn_exp2f(s0 - m[h]); p[1][h] = __builtin_amdgcn_exp2f(s1 - m[h]);
            lsum[h] = wave_sum(p[0][h] + p[1][h]);
          }
          P0 = pk_bf16(p[0][0], p[0][1]); P1 = pk_bf16(p[1][0], p[1][1]);
          if (l == 0) {
            *(LAS f32x4*)(L + L_ML + sw * 16) = f32x4{m[0], m[1], lsum[0], lsum[1]};
            CFENCE();
            *(volatile LAS unsigned*)(L + L_MLF + sw * 4) = 1u;
          }
          AFRAG(0);
          STAMP(5);
        }
      } else {
        acc = mfma16(afr, R[i], acc);
        if constexpr (i < 63) AFRAG(i - 31);
      }
      SB();
    };
    auto phase_b = [&](auto self, auto jc, auto dc) {
      constexpr int j = decltype(jc)::value, done = decltype(dc)::value;
      if constexpr (done < 64) {
        constexpr int landed = j < 64 ? j - WIN : 64;
        auto run = [&](auto self2, auto ic) {
          constexpr int i = decltype(ic)::value;
          if constexpr (i < 64 && i < done + CHUNK && pos(i < 64 ? i : 0) < landed) { process(ic); return self2(self2, std::integral_constant<int, i + 1>{}); }
          else return ic;
        };
        auto nd = run(run, std::integral_constant<int, done>{});
        if constexpr (j < 64) ISSUE(j);
        self(self, std::integral_constant<int, (j < 64 ? j + 1 : 64)>{}, nd);
      }
    };
    phase_b(phase_b, std::integral_constant<int, KB>{}, std::integral_constant<int, 0>{});
#undef COMB
#undef AFRAG
#undef ISSUE
    *(LAS f32x4*)(L + L_RED + sw * 1024 + (g >> 1) * 512 + c * 32 + (g & 1) * 16) = acc;
    STAMP(6);
    BARRIER();
    if (oproj) {
      const int off = (c & 7) * 1024 + sw * 128 + g * 32 + (c >> 3) * 16;
      __builtin_amdgcn_s_sleep(P_D1);
      SB();
      u32x4 a0 = ld_unit(rws, off, WS_OBOX + kvh * 16384), a1 = ld_unit(rws, off, WS_OBOX + kvh * 16384 + 8192);
      SB();
      u32x4 wfr[4];
#pragma unroll
      for (int t = 0; t < 4; ++t) wfr[t] = tr_frag(L + L_WO + (sw * 32 + 8 * g + (c >> 2)) * ROWP + t * 32 + (c & 3) * 8, 4 * ROWP);
      SB();
      {
        bool ok = (bad_unit(a0, ta, tb) | bad_unit(a1, ta, tb)) == 0u;
        for (unsigned spins = 0; !__all(ok) && spins < SPIN_CAP; ++spins) {
          CFENCE();
          if (!ok) {
            a0 = ld_unit(rws, off, WS_OBOX + kvh * 16384);
            a1 = ld_unit(rws, off, WS_OBOX + kvh * 16384 + 8192);
            ok = (bad_unit(a0, ta, tb) | bad_unit(a1, ta, tb)) == 0u;
          }
        }
      }
      STAMP(8);
      const bool lo8 = c < 8;
      const unsigned gx = dppu<XOR8>(lo8 ? a1.x : a0.x), gz = dppu<XOR8>(lo8 ? a1.z : a0.z);
      const u32x4 of = {lo8 ? a0.x : gx, lo8 ? a0.z : gz, lo8 ? gx : a1.x, lo8 ? gz : a1.z};
#pragma unroll
      for (int t = 0; t < 4; ++t) {
        const f32x4 d = mfma16(wfr[t], of, f32x4{0.f, 0.f, 0.f, 0.f});
        *(LAS f32x4*)(L + L_RED2 + (sw * 4 + t) * 1024 + l16) = d;
      }
      BARRIER();
      if (sw < 4) {
        f32x4 s = *(LAS f32x4*)(L + L_RED2 + sw * 1024 + l16);
#pragma unroll
        for (int ww = 1; ww < 8; ++ww) s += *(LAS f32x4*)(L + L_RED2 + (ww * 4 + sw) * 1024 + l16);
        const bool lo = c < 8;
        const unsigned v0 = __float_as_uint(s.x), v1 = __float_as_uint(s.y), n0 = dppu<XOR8>(__float_as_uint(s.z)), n1 = dppu<XOR8>(__float_as_uint(s.w));
        const int base = WS_PBOX + (((c & 7) * 16 + kvh) * 288 + b * 32 + sw * 8 + g * 2) * 16;
        st_unit(rws, base + (lo ? 0 : 16), u32x4{lo ? v0 : n0, ta, lo ? v1 : n1, tb});
        st_unit(rws, base + 8 * 16 * 288 * 16 + (lo ? 16 : 0), u32x4{lo ? n0 : v0, ta, lo ? n1 : v1, tb});
        STAMP(9);
      } else {
        const int poff = ((kvh * 16 + (l >> 3)) * 288 + b * 32 + (sw - 4) * 8 + (l & 7)) * 16;
        __builtin_amdgcn_s_sleep(P_D2);
        u32x4 p0 = ld_unit(rws, poff, WS_PBOX), p1 = ld_unit(rws, poff + 8 * 288 * 16, WS_PBOX);
        bool ok0 = bad_unit(p0, ta, tb) == 0u, ok1 = bad_unit(p1, ta, tb) == 0u;
        for (unsigned spins = 0; !__all(ok0 && ok1) && spins < SPIN_CAP; ++spins) {
          CFENCE();
          if (!ok0) p0 = ld_unit(rws, poff, WS_PBOX);
          if (!ok1) p1 = ld_unit(rws, poff + 8 * 288 * 16, WS_PBOX);
          ok0 = bad_unit(p0, ta, tb) == 0u; ok1 = bad_unit(p1, ta, tb) == 0u;
        }
        float v0 = __uint_as_float(p0.x) + __uint_as_float(p1.x), v1 = __uint_as_float(p0.z) + __uint_as_float(p1.z);
        v0 += dpp<XOR8>(v0); v1 += dpp<XOR8>(v1);
        v0 = xrow_sum(v0); v1 = xrow_sum(v1);
        if (l < 8) *(unsigned*)(out + kvh * D + b * 64 + (sw - 4) * 16 + l * 2) = pk_bf16(v0, v1);
        if (bid == 0 && tid == 5 * 64) __builtin_amdgcn_raw_buffer_store_b64(u32x2{ta, (unsigned)(e1 >> 32)}, rws, 0, WS_EPOCH, AUX_SC1);
        pace_until((unsigned)__builtin_amdgcn_s_memrealtime() + (unsigned)min(max((int)((unsigned)t3_ - (unsigned)t2_), 0) * 10, 5000));
      }
    }
  }
  asm volatile(".Lfused_code_end:");
}

extern "C" void kernel_launch(void* const* d_in, const int* in_sizes, int n_in, void* d_out, int out_size, void* d_ws,
                              size_t ws_size, hipStream_t stream) {
  fused_kernel<<<256, NWAVES * 64, 0, stream>>>((const unsigned short*)d_in[0], (const unsigned short*)d_in[1], (const unsigned short*)d_in[2],
                                                (const unsigned short*)d_in[3], (const unsigned short*)d_in[4], (unsigned short*)d_out,
                                                (unsigned char*)d_ws);
}
```

```cpp
#include <hip/hip_runtime.h>
#include <hip/hip_bf16.h>
#include <cstdint>
#include <type_traits>

#define DIAG 0
#define P_PACE 118
#define P_PACE0 170
#define P_PACE1 155
#define P_TPOLL 645
#define P_D1 14
#define P_D2 25

#define P_J1 64
#define P_PACEB 120
constexpr int due(int j) { return (j <= P_J1 ? j * P_PACE : P_J1 * P_PACE + (j - P_J1) * P_PACEB) >> 2; }
constexpr int B = 16, D = 576, HQ = 32, HKV = 16, SK = 4096, DH = 128, W = 1024;
constexpr int NQ = HQ * DH;
constexpr int NWAVES = 9;
constexpr int W0 = 4;
constexpr int WIN = 6;
constexpr int KB = 30;
constexpr int CHUNK = 3;

typedef unsigned u32x4 __attribute__((ext_vector_type(4)));
typedef unsigned u32x2 __attribute__((ext_vector_type(2)));
typedef float f32x4 __attribute__((ext_vector_type(4)));
typedef short bf16x8 __attribute__((ext_vector_type(8)));
typedef short s16x4 __attribute__((ext_vector_type(4)));
typedef __bf16 bf16x2 __attribute__((ext_vector_type(2)));
#define LAS __attribute__((address_space(3)))

constexpr int AUX_SC1 = 16, AUX_NT = 2;
constexpr int RSRC_W3 = 0x00020000;

constexpr int WS_EPOCH = 0;
constexpr int WS_TBOX = 1024;
constexpr int WS_QBOX = 4096;
constexpr int WS_OBOX = WS_QBOX + 16 * 16 * 1024;
constexpr int WS_PBOX = WS_OBOX + 16 * 16 * 1024;
constexpr int WS_END = WS_PBOX + 16 * 16 * 288 * 16;
constexpr int WS_DIAG = 8 << 20;
constexpr unsigned SPIN_CAP = 1u << 18;
constexpr int bitrev4(int i) { return ((i & 1) << 3) | ((i & 2) << 1) | ((i & 4) >> 1) | ((i & 8) >> 3); }

constexpr int ROWP = 144;
constexpr int L_RED = 0;
constexpr int L_ML = 8192;
constexpr int L_Q = L_ML + 128;
constexpr int L_FLAG = L_Q + 512;
constexpr int L_TREF = L_FLAG + 4;
constexpr int L_MLF = L_FLAG + 16;
constexpr int L_RED2 = 9216;
constexpr int L_WQ = L_RED2;
constexpr int L_WO = L_RED2 + 32768;
constexpr int L_BYTES = L_WO + 256 * ROWP;

__device__ __forceinline__ float dot2(unsigned k, unsigned q, float acc) {
  return __builtin_amdgcn_fdot2_f32_bf16(__builtin_bit_cast(bf16x2, k), __builtin_bit_cast(bf16x2, q), acc, false);
}
__device__ __forceinline__ unsigned pk_bf16(float lo, float hi) {
  bf16x2 v; v.x = (__bf16)lo; v.y = (__bf16)hi;
  return __builtin_bit_cast(unsigned, v);
}
template <int CTRL> __device__ __forceinline__ float dpp(float x) {
  return __builtin_bit_cast(float, __builtin_amdgcn_mov_dpp(__builtin_bit_cast(int, x), CTRL, 0xf, 0xf, true));
}
template <int CTRL> __device__ __forceinline__ unsigned dppu(unsigned x) {
  return (unsigned)__builtin_amdgcn_mov_dpp((int)x, CTRL, 0xf, 0xf, true);
}
constexpr int XOR1 = 0xB1, XOR2 = 0x4E, XOR7 = 0x141, XOR8 = 0x128, BCAST = 0x150;
__device__ __forceinline__ float xrow_max(float x) {
  auto s = __builtin_amdgcn_permlane16_swap(__float_as_uint(x), __float_as_uint(x), false, false);
  x = fmaxf(__uint_as_float(s[0]), __uint_as_float(s[1]));
  auto t = __builtin_amdgcn_permlane32_swap(__float_as_uint(x), __float_as_uint(x), false, false);
  return fmaxf(__uint_as_float(t[0]), __uint_as_float(t[1]));
}
__device__ __forceinline__ float xrow_sum(float x) {
  auto s = __builtin_amdgcn_permlane16_swap(__float_as_uint(x), __float_as_uint(x), false, false);
  x = __uint_as_float(s[0]) + __uint_as_float(s[1]);
  auto t = __builtin_amdgcn_permlane32_swap(__float_as_uint(x), __float_as_uint(x), false, false);
  return __uint_as_float(t[0]) + __uint_as_float(t[1]);
}
__device__ __forceinline__ float wave_max(float x) {
  x = fmaxf(x, dpp<XOR1>(x)); x = fmaxf(x, dpp<XOR2>(x)); x = fmaxf(x, dpp<XOR7>(x)); x = fmaxf(x, dpp<XOR8>(x));
  return xrow_max(x);
}
__device__ __forceinline__ float wave_sum(float x) {
  x += dpp<XOR1>(x); x += dpp<XOR2>(x); x += dpp<XOR7>(x); x += dpp<XOR8>(x);
  return xrow_sum(x);
}
__device__ __forceinline__ f32x4 mfma16(u32x4 a, u32x4 b, f32x4 c) {
  return __builtin_amdgcn_mfma_f32_16x16x32_bf16(__builtin_bit_cast(bf16x8, a), __builtin_bit_cast(bf16x8, b), c, 0, 0, 0);
}
__device__ __forceinline__ __amdgpu_buffer_rsrc_t make_rsrc(const void* p, int bytes) {
  return __builtin_amdgcn_make_buffer_rsrc((void*)p, 0, bytes, RSRC_W3);
}
__device__ __forceinline__ u32x4 tr_frag(LAS unsigned char* p, int second) {
  s16x4 lo = __builtin_amdgcn_ds_read_tr16_b64_v4i16((LAS s16x4*)p);
  s16x4 hi = __builtin_amdgcn_ds_read_tr16_b64_v4i16((LAS s16x4*)(p + second));
  u32x2 a = __builtin_bit_cast(u32x2, lo), b = __builtin_bit_cast(u32x2, hi);
  return u32x4{a.x, a.y, b.x, b.y};
}
__device__ __forceinline__ unsigned bad_unit(u32x4 v, unsigned ta, unsigned tb) { return (v.y ^ ta) | (v.w ^ tb); }
__device__ __forceinline__ void st_unit(__amdgpu_buffer_rsrc_t r, int off, u32x4 v) { __builtin_amdgcn_raw_buffer_store_b128(v, r, off, 0, AUX_SC1); }
__device__ __forceinline__ u32x4 ld_unit(__amdgpu_buffer_rsrc_t r, int off, int soff) { return __builtin_amdgcn_raw_buffer_load_b128(r, off, soff, AUX_SC1); }
#define SB() __builtin_amdgcn_sched_barrier(0)
#define CFENCE() asm volatile("" ::: "memory")
#define BARRIER() do { CFENCE(); asm volatile("s_waitcnt lgkmcnt(0)" ::: "memory"); __builtin_amdgcn_s_barrier(); CFENCE(); SB(); } while (0)
template <int N> __device__ __forceinline__ void wait_vm() { asm volatile("s_waitcnt vmcnt(%0)" ::"n"(N) : "memory"); }
__device__ __forceinline__ void pace_until(unsigned due) {
  asm volatile("1:\n\ts_memrealtime s[96:97]\n\ts_waitcnt lgkmcnt(0)\n\ts_sub_u32 s96, s96, %0\n\ts_cmp_lt_i32 s96, 0\n\ts_cbranch_scc1 1b"
               :: "s"(due) : "s96", "s97", "scc", "memory");
}

#define STAMP(i) do { } while (0)

__global__ void __launch_bounds__(NWAVES * 64, 3)
fused_kernel(const unsigned short* __restrict__ x, const unsigned short* __restrict__ kc, const unsigned short* __restrict__ vc,
             const unsigned short* __restrict__ wq, const unsigned short* __restrict__ wo, unsigned short* __restrict__ out,
             unsigned char* __restrict__ ws) {
  __shared__ __attribute__((aligned(16))) unsigned char smem[L_BYTES];
  LAS unsigned char* L = (LAS unsigned char*)smem;
  const int bid = blockIdx.x;
  const int kvh = (bid & 7) * 2 + ((bid >> 3) & 1), b = bid >> 4;
  const int tid = threadIdx.x, l = tid & 63, w = __builtin_amdgcn_readfirstlane(tid >> 6), g = l >> 4, c = l & 15;
  const int sw = w - 1;
  STAMP(0);
  unsigned pf;
  {
    unsigned long long pc; unsigned clen;
    asm volatile("s_getpc_b64 %0\n.Lfused_code_begin:\n\ts_mov_b32 %1, .Lfused_code_end-.Lfused_code_begin" : "=s"(pc), "=s"(clen));
    pf = __builtin_amdgcn_raw_buffer_load_b32(make_rsrc((const void*)pc, (int)clen), tid < 8 ? (tid * 32 + (bid >> 3)) * 128 : 0x7fff0000, 0, 0);
  }
  *(LAS unsigned*)(L + L_FLAG + (l & 15) * 4) = 0u;
  const bool oproj = b < 9;
  const int offa_s = ((b * HKV + kvh) * SK + (SK - W) - 4) * (DH * 2) + tid * 16, offa_q = (l >> 1) * (NQ * 2) + (l & 1) * 16 + (kvh * 256 + b * 16) * 2;
  const int offb_s = (kvh * 256 - 32 + (tid >> 6) * 32 + (l >> 3)) * (D * 2) + (oproj ? b * 128 : 0x7f000000) + (l & 7) * 16, offb_q = c * (D * 2) + g * 16;
  int offa = tid < 64 ? offa_q : offa_s, offb = tid < 64 ? offb_q : offb_s;
  const int rot8k = (bid >> 3) * 8192;
  asm volatile("" : "+v"(offa), "+v"(offb));
  SB();
  asm volatile("" ::"s"(x), "s"(kc), "s"(vc), "s"(wq), "s"(wo), "s"(out), "s"(ws));
  const unsigned t0 = (unsigned)__builtin_amdgcn_s_memrealtime();
  SB();
  const __amdgpu_buffer_rsrc_t rws = make_rsrc(ws, WS_END);
  const int l16 = l * 16;
  unsigned long long e1;
  unsigned ta, tb;
#define EPOCH_GET() do { asm volatile("s_load_dwordx2 %0, %1, 0x0\n\ts_waitcnt lgkmcnt(0)" : "=s"(e1) : "s"(ws + WS_EPOCH) : "memory"); SB(); \
    e1 += 1ull; ta = (unsigned)e1; tb = (unsigned)(e1 >> 32) ^ (ta * 0x9E3779B1u); } while (0)

  if (w == 0) {
    __builtin_amdgcn_s_setprio(3);
    {
      const __amdgpu_buffer_rsrc_t rwq = make_rsrc(wq, D * NQ * 2);
      const __amdgpu_buffer_rsrc_t rx = make_rsrc(x, B * D * 2);
      u32x4 wr[18], xr[18];
#pragma unroll
      for (int s = 0; s < 18; ++s) {
        wr[s] = __builtin_amdgcn_raw_buffer_load_b128(rwq, offa, s * 32 * NQ * 2, AUX_NT); SB();
        xr[s] = __builtin_amdgcn_raw_buffer_load_b128(rx, offb + s * 64, 0, 0); SB();
        if (s == 2) BARRIER();
      }
      EPOCH_GET();
      if (bid < 8 && l < 8) st_unit(rws, WS_TBOX + bid * 128 + l16, u32x4{t0, ta, t0, tb});
      STAMP(1);
#pragma unroll
      for (int s = 0; s < 18; ++s) *(LAS u32x4*)(L + L_WQ + s * 1024 + l16) = wr[s];
      f32x4 acc = {0.f, 0.f, 0.f, 0.f};
      const int troff = (8 * g + (c >> 2)) * 32 + (c & 3) * 8;
#pragma unroll
      for (int s = 0; s < 18; ++s) acc = mfma16(tr_frag(L + L_WQ + s * 1024 + troff, 128), xr[s], acc);
      STAMP(2);
      st_unit(rws, WS_QBOX + kvh * 16384 + c * 1024 + (b * 4 + g) * 16, u32x4{pk_bf16(acc[0], acc[1]), ta, pk_bf16(acc[2], acc[3]), tb});
      STAMP(3);
    }
    asm volatile("" ::"v"(pf));
    while ((unsigned)__builtin_amdgcn_s_memrealtime() - t0 < (unsigned)P_TPOLL) __builtin_amdgcn_s_sleep(4);
    {
      const u32x4 tu = ld_unit(rws, (l & 7) * 128, WS_TBOX);
      u32x4 qa = ld_unit(rws, l16, WS_QBOX + (kvh * 16 + b) * 1024);
      int d = bad_unit(tu, ta, tb) == 0u ? (int)(tu.x - t0) : 0;
      d = min(max(d, -300), 300);
      d += __builtin_amdgcn_mov_dpp(d, XOR1, 0xf, 0xf, true); d += __builtin_amdgcn_mov_dpp(d, XOR2, 0xf, 0xf, true);
      d += __builtin_amdgcn_mov_dpp(d, XOR7, 0xf, 0xf, true); d >>= 3;
      *(LAS unsigned*)(L + L_TREF) = t0 + (unsigned)d;
      for (unsigned spins = 0; !__all(bad_unit(qa, ta, tb) == 0u) && spins < SPIN_CAP; ++spins) {
        __builtin_amdgcn_s_sleep(8);
        CFENCE();
        qa = ld_unit(rws, l16, WS_QBOX + (kvh * 16 + b) * 1024);
      }
      *(LAS u32x2*)(L + L_Q + l * 8) = u32x2{qa.x, qa.z};
      asm volatile("s_waitcnt lgkmcnt(0)" ::: "memory");
      *(volatile LAS unsigned*)(L + L_FLAG) = 1u;
    }
    STAMP(4);
    __builtin_amdgcn_s_setprio(0);
    for (unsigned spins = 0; !__all(*(volatile LAS unsigned*)(L + L_MLF + (l & 7) * 4) != 0u) && spins < (SPIN_CAP << 4); ++spins) __builtin_amdgcn_s_sleep(16);
    CFENCE();
    float fw[8];
    {
      const int h = l >> 5;
      float mw[8], lw[8];
#pragma unroll
      for (int ww = 0; ww < 8; ++ww) { mw[ww] = *(LAS float*)(L + L_ML + ww * 16 + h * 4); lw[ww] = *(LAS float*)(L + L_ML + ww * 16 + 8 + h * 4); }
      float M = mw[0];
#pragma unroll
      for (int ww = 1; ww < 8; ++ww) M = fmaxf(M, mw[ww]);
      float Ls = 0.f;
#pragma unroll
      for (int ww = 0; ww < 8; ++ww) { fw[ww] = __builtin_amdgcn_exp2f(mw[ww] - M); Ls = fmaf(fw[ww], lw[ww], Ls); }
      const float inv = 1.0f / Ls;
#pragma unroll
      for (int ww = 0; ww < 8; ++ww) { fw[ww] *= inv; asm volatile("" : "+v"(fw[ww])); }
    }
    int ooff = WS_OBOX + (kvh * 16 + b) * 1024 + l16;
    asm volatile("" : "+v"(ooff));
    __builtin_amdgcn_s_setprio(3);
    BARRIER();
    STAMP(6);
    f32x4 ow[8];
#pragma unroll
    for (int ww = 0; ww < 8; ++ww) ow[ww] = *(LAS f32x4*)(L + L_RED + ww * 1024 + l16);
    const f32x4 O = ((fw[0] * ow[0] + fw[1] * ow[1]) + (fw[2] * ow[2] + fw[3] * ow[3])) + ((fw[4] * ow[4] + fw[5] * ow[5]) + (fw[6] * ow[6] + fw[7] * ow[7]));
    st_unit(rws, ooff, u32x4{pk_bf16(O.x, O.y), ta, pk_bf16(O.z, O.w), tb});
    STAMP(7);
    if (oproj) BARRIER();
  } else {
    const __amdgpu_buffer_rsrc_t rk = make_rsrc(kc, B * HKV * SK * DH * 2);
    const __amdgpu_buffer_rsrc_t rv = make_rsrc(vc, B * HKV * SK * DH * 2);
    unsigned tp0 = 0;
    u32x4 R[64];
    const __amdgpu_buffer_rsrc_t rwo = make_rsrc(wo, NQ * D * 2);
    u32x4 wor[4];
    constexpr auto item = [](int j) constexpr { return j == 0 ? 0 : (j == 1 ? 32 : (j <= 32 ? j - 1 : j)); };
    constexpr auto pos = [](int i) constexpr { return i == 0 ? 0 : (i == 32 ? 1 : (i < 32 ? i + 1 : i)); };
#define ISSUE(j) do { constexpr int it_ = item(j);                                                                               \
      if constexpr ((j) >= W0) { wait_vm<WIN - 1>(); pace_until(tp0 + (unsigned)due(j)); SB(); }                                 \
      R[it_] = __builtin_amdgcn_raw_buffer_load_b128(it_ < 32 ? rk : rv, offa, (it_ * 8192 + rot8k) & 0x3ffff, AUX_NT); SB(); } while (0)
    auto phase_a = [&](auto self, auto jc) {
      constexpr int j = decltype(jc)::value;
      if constexpr (j < KB) {
        ISSUE(j);
        if constexpr (j == 1) {
#pragma unroll
          for (int u = 0; u < 4; ++u) { wor[u] = __builtin_amdgcn_raw_buffer_load_b128(rwo, offb, u * 8 * D * 2, AUX_NT); SB(); }
        }
        if constexpr (j == W0 - 1) {
          BARRIER(); EPOCH_GET();
          tp0 = t0; asm volatile("" : "+s"(tp0)); tp0 += (unsigned)(P_PACE0 + ((sw * P_PACE) >> 5));
        }
        if constexpr (j == W0) {
#pragma unroll
          for (int u = 0; u < 4; ++u) *(LAS u32x4*)(L + L_WO + (sw * 32 + u * 8 + (l >> 3)) * ROWP + (l & 7) * 16) = wor[u];
          SB();
        }
        self(self, std::integral_constant<int, j + 1>{});
      }
    };
    phase_a(phase_a, std::integral_constant<int, 0>{});
    STAMP(1);
    asm volatile("" ::"v"(pf));
    for (unsigned spins = 0; *(volatile LAS unsigned*)(L + L_FLAG) == 0u && spins < (SPIN_CAP << 4); ++spins) __builtin_amdgcn_s_sleep(1);
    CFENCE();
    STAMP(4);
    {
      const int dt = (int)((unsigned)__builtin_amdgcn_readfirstlane(*(LAS unsigned*)(L + L_TREF)) - t0);
      tp0 = t0 + (unsigned)(min(max(dt, -400), 400) + P_PACE1 + ((sw * P_PACE) >> 5));
    }
    const u32x4 q0 = *(LAS u32x4*)(L + L_Q + c * 16), q1 = *(LAS u32x4*)(L + L_Q + 256 + c * 16);
    const bool up8 = (c & 8) != 0, up4 = (c & 4) != 0, up2 = (c & 2) != 0, up1 = (c & 1) != 0;
    float u0[4], u1[4], sc[2][2];
    const int hh = c >> 3, jj = c & 7;
    const unsigned pick = (unsigned)((2 * hh + 1) << 8 | (2 * hh));
    const unsigned act = (jj & 1) ? (pick << 16 | 0x0C0Cu) : (0x0C0C0000u | pick);
    u32x4 sel;
    sel.x = (jj >> 1) == 0 ? act : 0x0C0C0C0Cu; sel.y = (jj >> 1) == 1 ? act : 0x0C0C0C0Cu;
    sel.z = (jj >> 1) == 2 ? act : 0x0C0C0C0Cu; sel.w = (jj >> 1) == 3 ? act : 0x0C0C0C0Cu;
    unsigned P0 = 0, P1 = 0;
    f32x4 acc = {0.f, 0.f, 0.f, 0.f};
    u32x4 afr = {0u, 0u, 0u, 0u};
    constexpr float C2 = 0.088388347648318440f * 1.4426950408889634f;
#define COMB(UP, CTRL, LO, HI) ((UP ? HI : LO) + dpp<CTRL>(UP ? LO : HI))
#define AFRAG(I) do { const unsigned t_ = dppu<BCAST + bitrev4((I) & 15)>((I) < 16 ? P0 : P1);                                   \
      afr.x = __builtin_amdgcn_perm(t_, t_, sel.x); afr.y = __builtin_amdgcn_perm(t_, t_, sel.y);                                \
      afr.z = __builtin_amdgcn_perm(t_, t_, sel.z); afr.w = __builtin_amdgcn_perm(t_, t_, sel.w); } while (0)
    auto process = [&](auto ic) {
      constexpr int i = decltype(ic)::value;
      if constexpr (i < 32) {
        const u32x4 kk = R[i];
        float a = dot2(kk.x, q0.x, 0.f), bb = dot2(kk.x, q1.x, 0.f);
        a = dot2(kk.y, q0.y, a); bb = dot2(kk.y, q1.y, bb);
        a = dot2(kk.z, q0.z, a); bb = dot2(kk.z, q1.z, bb);
        a = dot2(kk.w, q0.w, a); bb = dot2(kk.w, q1.w, bb);
        if constexpr (!(i & 1)) { u0[0] = a; u1[0] = bb; }
        else {
          a = COMB(up8, XOR8, u0[0], a); bb = COMB(up8, XOR8, u1[0], bb);
          if constexpr (!(i & 2)) { u0[1] = a; u1[1] = bb; }
          else {
            a = COMB(up4, XOR7, u0[1], a); bb = COMB(up4, XOR7, u1[1], bb);
            if constexpr (!(i & 4)) { u0[2] = a; u1[2] = bb; }
            else {
              a = COMB(up2, XOR2, u0[2], a); bb = COMB(up2, XOR2, u1[2], bb);
              if constexpr (!(i & 8)) { u0[3] = a; u1[3] = bb; }
              else {
                a = COMB(up1, XOR1, u0[3], a); bb = COMB(up1, XOR1, u1[3], bb);
                sc[i >> 4][0] = a; sc[i >> 4][1] = bb;
              }
            }
          }
        }
        if constexpr (i == 31) {
          float m[2], lsum[2], p[2][2];
#pragma unroll
          for (int h = 0; h < 2; ++h) {
            const float s0 = sc[0][h] * C2, s1 = sc[1][h] * C2;
            m[h] = wave_max(fmaxf(s0, s1));
            p[0][h] = __builtin_amdgcn_exp2f(s0 - m[h]); p[1][h] = __builtin_amdgcn_exp2f(s1 - m[h]);
            lsum[h] = wave_sum(p[0][h] + p[1][h]);
          }
          P0 = pk_bf16(p[0][0], p[0][1]); P1 = pk_bf16(p[1][0], p[1][1]);
          if (l == 0) {
            *(LAS f32x4*)(L + L_ML + sw * 16) = f32x4{m[0], m[1], lsum[0], lsum[1]};
            CFENCE();
            *(volatile LAS unsigned*)(L + L_MLF + sw * 4) = 1u;
          }
          AFRAG(0);
          STAMP(5);
        }
      } else {
        acc = mfma16(afr, R[i], acc);
        if constexpr (i < 63) AFRAG(i - 31);
      }
      SB();
    };
    auto phase_b = [&](auto self, auto jc, auto dc) {
      constexpr int j = decltype(jc)::value, done = decltype(dc)::value;
      if constexpr (done < 64) {
        constexpr int landed = j < 64 ? j - WIN : 64;
        auto run = [&](auto self2, auto ic) {
          constexpr int i = decltype(ic)::value;
          if constexpr (i < 64 && i < done + CHUNK && pos(i < 64 ? i : 0) < landed) { process(ic); return self2(self2, std::integral_constant<int, i + 1>{}); }
          else return ic;
        };
        auto nd = run(run, std::integral_constant<int, done>{});
        if constexpr (j < 64) ISSUE(j);
        self(self, std::integral_constant<int, (j < 64 ? j + 1 : 64)>{}, nd);
      }
    };
    phase_b(phase_b, std::integral_constant<int, KB>{}, std::integral_constant<int, 0>{});
#undef COMB
#undef AFRAG
#undef ISSUE
    *(LAS f32x4*)(L + L_RED + sw * 1024 + (g >> 1) * 512 + c * 32 + (g & 1) * 16) = acc;
    STAMP(6);
    BARRIER();
    if (oproj) {
      const int off = (c & 7) * 1024 + sw * 128 + g * 32 + (c >> 3) * 16;
      __builtin_amdgcn_s_sleep(P_D1);
      SB();
      u32x4 a0 = ld_unit(rws, off, WS_OBOX + kvh * 16384), a1 = ld_unit(rws, off, WS_OBOX + kvh * 16384 + 8192);
      SB();
      u32x4 wfr[4];
#pragma unroll
      for (int t = 0; t < 4; ++t) wfr[t] = tr_frag(L + L_WO + (sw * 32 + 8 * g + (c >> 2)) * ROWP + t * 32 + (c & 3) * 8, 4 * ROWP);
      SB();
      {
        bool ok = (bad_unit(a0, ta, tb) | bad_unit(a1, ta, tb)) == 0u;
        for (unsigned spins = 0; !__all(ok) && spins < SPIN_CAP; ++spins) {
          CFENCE();
          if (!ok) {
            a0 = ld_unit(rws, off, WS_OBOX + kvh * 16384);
            a1 = ld_unit(rws, off, WS_OBOX + kvh * 16384 + 8192);
            ok = (bad_unit(a0, ta, tb) | bad_unit(a1, ta, tb)) == 0u;
          }
        }
      }
      STAMP(8);
      const bool lo8 = c < 8;
      const unsigned gx = dppu<XOR8>(lo8 ? a1.x : a0.x), gz = dppu<XOR8>(lo8 ? a1.z : a0.z);
      const u32x4 of = {lo8 ? a0.x : gx, lo8 ? a0.z : gz, lo8 ? gx : a1.x, lo8 ? gz : a1.z};
#pragma unroll
      for (int t = 0; t < 4; ++t) {
        const f32x4 d = mfma16(wfr[t], of, f32x4{0.f, 0.f, 0.f, 0.f});
        *(LAS f32x4*)(L + L_RED2 + (sw * 4 + t) * 1024 + l16) = d;
      }
      BARRIER();
      if (sw < 4) {
        f32x4 s = *(LAS f32x4*)(L + L_RED2 + sw * 1024 + l16);
#pragma unroll
        for (int ww = 1; ww < 8; ++ww) s += *(LAS f32x4*)(L + L_RED2 + (ww * 4 + sw) * 1024 + l16);
        const bool lo = c < 8;
        const unsigned v0 = __float_as_uint(s.x), v1 = __float_as_uint(s.y), n0 = dppu<XOR8>(__float_as_uint(s.z)), n1 = dppu<XOR8>(__float_as_uint(s.w));
        const int base = WS_PBOX + (((c & 7) * 16 + kvh) * 288 + b * 32 + sw * 8 + g * 2) * 16;
        st_unit(rws, base + (lo ? 0 : 16), u32x4{lo ? v0 : n0, ta, lo ? v1 : n1, tb});
        st_unit(rws, base + 8 * 16 * 288 * 16 + (lo ? 16 : 0), u32x4{lo ? n0 : v0, ta, lo ? n1 : v1, tb});
        STAMP(9);
      } else {
        const int poff = ((kvh * 16 + (l >> 3)) * 288 + b * 32 + (sw - 4) * 8 + (l & 7)) * 16;
        __builtin_amdgcn_s_sleep(P_D2);
        u32x4 p0 = ld_unit(rws, poff, WS_PBOX), p1 = ld_unit(rws, poff + 8 * 288 * 16, WS_PBOX);
        bool ok0 = bad_unit(p0, ta, tb) == 0u, ok1 = bad_unit(p1, ta, tb) == 0u;
        for (unsigned spins = 0; !__all(ok0 && ok1) && spins < SPIN_CAP; ++spins) {
          CFENCE();
          if (!ok0) p0 = ld_unit(rws, poff, WS_PBOX);
          if (!ok1) p1 = ld_unit(rws, poff + 8 * 288 * 16, WS_PBOX);
          ok0 = bad_unit(p0, ta, tb) == 0u; ok1 = bad_unit(p1, ta, tb) == 0u;
        }
        float v0 = __uint_as_float(p0.x) + __uint_as_float(p1.x), v1 = __uint_as_float(p0.z) + __uint_as_float(p1.z);
        v0 += dpp<XOR8>(v0); v1 += dpp<XOR8>(v1);
        v0 = xrow_sum(v0); v1 = xrow_sum(v1);
        if (l < 8) *(unsigned*)(out + kvh * D + b * 64 + (sw - 4) * 16 + l * 2) = pk_bf16(v0, v1);
        if (bid == 0 && tid == 5 * 64) __builtin_amdgcn_raw_buffer_store_b64(u32x2{ta, (unsigned)(e1 >> 32)}, rws, 0, WS_EPOCH, AUX_SC1);
        STAMP(10);
      }
    }
  }
  asm volatile(".Lfused_code_end:");
}

extern "C" void kernel_launch(void* const* d_in, const int* in_sizes, int n_in, void* d_out, int out_size, void* d_ws,
                              size_t ws_size, hipStream_t stream) {
  fused_kernel<<<256, NWAVES * 64, 0, stream>>>((const unsigned short*)d_in[0], (const unsigned short*)d_in[1], (const unsigned short*)d_in[2],
                                                (const unsigned short*)d_in[3], (const unsigned short*)d_in[4], (unsigned short*)d_out,
                                                (unsigned char*)d_ws);
}
```

```cpp
#include <hip/hip_runtime.h>
#include <hip/hip_bf16.h>
#include <cstdint>
#include <type_traits>

constexpr int P_PACE = 118;
constexpr int P_PACE0 = 178;
constexpr int P_PACE1 = 162;
constexpr int P_TPOLL = 660;
constexpr int P_D1 = 14;
constexpr int P_D2 = 25;

constexpr int due(int j) { return (j * P_PACE) >> 2; }

constexpr int B = 16, D = 576, HQ = 32, HKV = 16, SK = 4096, DH = 128, W = 1024;
constexpr int NQ = HQ * DH;
constexpr int NWAVES = 9;
constexpr int W0 = 4;
constexpr int WIN = 6;
constexpr int KB = 30;
constexpr int CHUNK = 2;

typedef unsigned u32x4 __attribute__((ext_vector_type(4)));
typedef unsigned u32x2 __attribute__((ext_vector_type(2)));
typedef float f32x4 __attribute__((ext_vector_type(4)));
typedef short bf16x8 __attribute__((ext_vector_type(8)));
typedef short s16x4 __attribute__((ext_vector_type(4)));
typedef __bf16 bf16x2 __attribute__((ext_vector_type(2)));
#define LAS __attribute__((address_space(3)))

constexpr int AUX_SC1 = 16, AUX_NT = 2;
constexpr int RSRC_W3 = 0x00020000;

constexpr int WS_EPOCH = 0;
constexpr int WS_TBOX = 1024;
constexpr int WS_QBOX = 4096;
constexpr int WS_OBOX = WS_QBOX + 16 * 16 * 1024;
constexpr int WS_PBOX = WS_OBOX + 16 * 16 * 1024;
constexpr int WS_END = WS_PBOX + 16 * 16 * 288 * 16;
constexpr unsigned SPIN_CAP = 1u << 18;
constexpr int bitrev4(int i) { return ((i & 1) << 3) | ((i & 2) << 1) | ((i & 4) >> 1) | ((i & 8) >> 3); }

constexpr int ROWP = 144;
constexpr int L_RED = 0;
constexpr int L_ML = 8192;
constexpr int L_Q = L_ML + 128;
constexpr int L_FLAG = L_Q + 512;
constexpr int L_TREF = L_FLAG + 4;
constexpr int L_MLF = L_FLAG + 16;
constexpr int L_RED2 = 9216;
constexpr int L_WQ = L_RED2;
constexpr int L_WO = L_RED2 + 32768;
constexpr int L_BYTES = L_WO + 256 * ROWP;

__device__ __forceinline__ float dot2(unsigned k, unsigned q, float acc) {
  return __builtin_amdgcn_fdot2_f32_bf16(__builtin_bit_cast(bf16x2, k), __builtin_bit_cast(bf16x2, q), acc, false);
}
__device__ __forceinline__ unsigned pk_bf16(float lo, float hi) {
  bf16x2 v; v.x = (__bf16)lo; v.y = (__bf16)hi;
  return __builtin_bit_cast(unsigned, v);
}
template <int CTRL> __device__ __forceinline__ float dpp(float x) {
  return __builtin_bit_cast(float, __builtin_amdgcn_mov_dpp(__builtin_bit_cast(int, x), CTRL, 0xf, 0xf, true));
}
template <int CTRL> __device__ __forceinline__ unsigned dppu(unsigned x) {
  return (unsigned)__builtin_amdgcn_mov_dpp((int)x, CTRL, 0xf, 0xf, true);
}
constexpr int XOR1 = 0xB1, XOR2 = 0x4E, XOR7 = 0x141, XOR8 = 0x128, BCAST = 0x150;
__device__ __forceinline__ float xrow_max(float x) {
  auto s = __builtin_amdgcn_permlane16_swap(__float_as_uint(x), __float_as_uint(x), false, false);
  x = fmaxf(__uint_as_float(s[0]), __uint_as_float(s[1]));
  auto t = __builtin_amdgcn_permlane32_swap(__float_as_uint(x), __float_as_uint(x), false, false);
  return fmaxf(__uint_as_float(t[0]), __uint_as_float(t[1]));
}
__device__ __forceinline__ float xrow_sum(float x) {
  auto s = __builtin_amdgcn_permlane16_swap(__float_as_uint(x), __float_as_uint(x), false, false);
  x = __uint_as_float(s[0]) + __uint_as_float(s[1]);
  auto t = __builtin_amdgcn_permlane32_swap(__float_as_uint(x), __float_as_uint(x), false, false);
  return __uint_as_float(t[0]) + __uint_as_float(t[1]);
}
__device__ __forceinline__ float wave_max(float x) {
  x = fmaxf(x, dpp<XOR1>(x)); x = fmaxf(x, dpp<XOR2>(x)); x = fmaxf(x, dpp<XOR7>(x)); x = fmaxf(x, dpp<XOR8>(x));
  return xrow_max(x);
}
__device__ __forceinline__ float wave_sum(float x) {
  x += dpp<XOR1>(x); x += dpp<XOR2>(x); x += dpp<XOR7>(x); x += dpp<XOR8>(x);
  return xrow_sum(x);
}
__device__ __forceinline__ f32x4 mfma16(u32x4 a, u32x4 b, f32x4 c) {
  return __builtin_amdgcn_mfma_f32_16x16x32_bf16(__builtin_bit_cast(bf16x8, a), __builtin_bit_cast(bf16x8, b), c, 0, 0, 0);
}
__device__ __forceinline__ __amdgpu_buffer_rsrc_t make_rsrc(const void* p, int bytes) {
  return __builtin_amdgcn_make_buffer_rsrc((void*)p, 0, bytes, RSRC_W3);
}
__device__ __forceinline__ u32x4 tr_frag(LAS unsigned char* p, int second) {
  s16x4 lo = __builtin_amdgcn_ds_read_tr16_b64_v4i16((LAS s16x4*)p);
  s16x4 hi = __builtin_amdgcn_ds_read_tr16_b64_v4i16((LAS s16x4*)(p + second));
  u32x2 a = __builtin_bit_cast(u32x2, lo), b = __builtin_bit_cast(u32x2, hi);
  return u32x4{a.x, a.y, b.x, b.y};
}
__device__ __forceinline__ unsigned bad_unit(u32x4 v, unsigned ta, unsigned tb) { return (v.y ^ ta) | (v.w ^ tb); }
__device__ __forceinline__ void st_unit(__amdgpu_buffer_rsrc_t r, int off, u32x4 v) { __builtin_amdgcn_raw_buffer_store_b128(v, r, off, 0, AUX_SC1); }
__device__ __forceinline__ u32x4 ld_unit(__amdgpu_buffer_rsrc_t r, int off, int soff) { return __builtin_amdgcn_raw_buffer_load_b128(r, off, soff, AUX_SC1); }
#define SB() __builtin_amdgcn_sched_barrier(0)
#define CFENCE() asm volatile("" ::: "memory")
#define BARRIER() do { CFENCE(); asm volatile("s_waitcnt lgkmcnt(0)" ::: "memory"); __builtin_amdgcn_s_barrier(); CFENCE(); SB(); } while (0)
template <int N> __device__ __forceinline__ void wait_vm() { asm volatile("s_waitcnt vmcnt(%0)" ::"n"(N) : "memory"); }
__device__ __forceinline__ void pace_until(unsigned due) {
  asm volatile("1:\n\ts_memrealtime s[96:97]\n\ts_waitcnt lgkmcnt(0)\n\ts_sub_u32 s96, s96, %0\n\ts_cmp_lt_i32 s96, 0\n\ts_cbranch_scc1 1b"
               :: "s"(due) : "s96", "s97", "scc", "memory");
}

__global__ void __launch_bounds__(NWAVES * 64, 3)
fused_kernel(const unsigned short* __restrict__ x, const unsigned short* __restrict__ kc, const unsigned short* __restrict__ vc,
             const unsigned short* __restrict__ wq, const unsigned short* __restrict__ wo, unsigned short* __restrict__ out,
             unsigned char* __restrict__ ws) {
  __shared__ __attribute__((aligned(16))) unsigned char smem[L_BYTES];
  LAS unsigned char* L = (LAS unsigned char*)smem;
  const int bid = blockIdx.x;
  const int kvh = (bid & 7) * 2 + ((bid >> 3) & 1), b = bid >> 4;
  const int tid = threadIdx.x, l = tid & 63, w = __builtin_amdgcn_readfirstlane(tid >> 6), g = l >> 4, c = l & 15;
  const int sw = w - 1;
  unsigned pf;
  {
    unsigned long long pc; unsigned clen;
    asm volatile("s_getpc_b64 %0\n.Lfused_code_begin:\n\ts_mov_b32 %1, .Lfused_code_end-.Lfused_code_begin" : "=s"(pc), "=s"(clen));
    pf = __builtin_amdgcn_raw_buffer_load_b32(make_rsrc((const void*)pc, (int)clen), tid < 8 ? (tid * 32 + (bid >> 3)) * 128 : 0x7fff0000, 0, 0);
  }
  unsigned long long t64 = __builtin_amdgcn_s_memrealtime();
  *(LAS unsigned*)(L + L_FLAG + (l & 15) * 4) = 0u;
  const bool oproj = b < 9;
  const int offa_s = ((b * HKV + kvh) * SK + (SK - W) - 4) * (DH * 2) + tid * 16, offa_q = (l >> 1) * (NQ * 2) + (l & 1) * 16 + (kvh * 256 + b * 16) * 2;
  const int offb_s = (kvh * 256 - 32 + (tid >> 6) * 32 + (l >> 3)) * (D * 2) + (oproj ? b * 128 : 0x7f000000) + (l & 7) * 16, offb_q = c * (D * 2) + g * 16;
  int offa = tid < 64 ? offa_q : offa_s, offb = tid < 64 ? offb_q : offb_s;
  const int rot8k = (bid >> 3) * 8192;
  asm volatile("" : "+v"(offa), "+v"(offb));
  SB();
  asm volatile("" : "+s"(t64) : "s"(x), "s"(kc), "s"(vc), "s"(wq), "s"(wo), "s"(out), "s"(ws));
  const unsigned t0 = (unsigned)t64;
  const __amdgpu_buffer_rsrc_t rws = make_rsrc(ws, WS_END);
  const int l16 = l * 16;
  unsigned long long e1;
  unsigned ta, tb;
#define EPOCH_GET() do { asm volatile("s_load_dwordx2 %0, %1, 0x0\n\ts_waitcnt lgkmcnt(0)" : "=s"(e1) : "s"(ws + WS_EPOCH) : "memory"); SB(); \
    e1 += 1ull; ta = (unsigned)e1; tb = (unsigned)(e1 >> 32) ^ (ta * 0x9E3779B1u); } while (0)

  if (w == 0) {
    __builtin_amdgcn_s_setprio(3);
    {
      const __amdgpu_buffer_rsrc_t rwq = make_rsrc(wq, D * NQ * 2);
      const __amdgpu_buffer_rsrc_t rx = make_rsrc(x, B * D * 2);
      u32x4 wr[18], xr[18];
#pragma unroll
      for (int s = 0; s < 18; ++s) {
        wr[s] = __builtin_amdgcn_raw_buffer_load_b128(rwq, offa, s * 32 * NQ * 2, AUX_NT); SB();
        xr[s] = __builtin_amdgcn_raw_buffer_load_b128(rx, offb + s * 64, 0, 0); SB();
        if (s == 2) BARRIER();
      }
      EPOCH_GET();
      if (bid < 8 && l < 8) st_unit(rws, WS_TBOX + bid * 128 + l16, u32x4{t0, ta, t0, tb});
#pragma unroll
      for (int s = 0; s < 18; ++s) *(LAS u32x4*)(L + L_WQ + s * 1024 + l16) = wr[s];
      f32x4 acc = {0.f, 0.f, 0.f, 0.f};
      const int troff = (8 * g + (c >> 2)) * 32 + (c & 3) * 8;
#pragma unroll
      for (int s = 0; s < 18; ++s) acc = mfma16(tr_frag(L + L_WQ + s * 1024 + troff, 128), xr[s], acc);
      st_unit(rws, WS_QBOX + kvh * 16384 + c * 1024 + (b * 4 + g) * 16, u32x4{pk_bf16(acc[0], acc[1]), ta, pk_bf16(acc[2], acc[3]), tb});
    }
    asm volatile("" ::"v"(pf));
    while ((unsigned)__builtin_amdgcn_s_memrealtime() - t0 < (unsigned)P_TPOLL) __builtin_amdgcn_s_sleep(4);
    {
      const u32x4 tu = ld_unit(rws, (l & 7) * 128, WS_TBOX);
      u32x4 qa = ld_unit(rws, l16, WS_QBOX + (kvh * 16 + b) * 1024);
      int d = bad_unit(tu, ta, tb) == 0u ? (int)(tu.x - t0) : 0;
      d = min(max(d, -300), 300);
      d += __builtin_amdgcn_mov_dpp(d, XOR1, 0xf, 0xf, true); d += __builtin_amdgcn_mov_dpp(d, XOR2, 0xf, 0xf, true);
      d += __builtin_amdgcn_mov_dpp(d, XOR7, 0xf, 0xf, true); d >>= 3;
      *(LAS unsigned*)(L + L_TREF) = t0 + (unsigned)d;
      for (unsigned spins = 0; !__all(bad_unit(qa, ta, tb) == 0u) && spins < SPIN_CAP; ++spins) {
        __builtin_amdgcn_s_sleep(8);
        CFENCE();
        qa = ld_unit(rws, l16, WS_QBOX + (kvh * 16 + b) * 1024);
      }
      *(LAS u32x2*)(L + L_Q + l * 8) = u32x2{qa.x, qa.z};
      asm volatile("s_waitcnt lgkmcnt(0)" ::: "memory");
      *(volatile LAS unsigned*)(L + L_FLAG) = 1u;
    }
    __builtin_amdgcn_s_setprio(0);
    for (unsigned spins = 0; !__all(*(volatile LAS unsigned*)(L + L_MLF + (l & 7) * 4) != 0u) && spins < (SPIN_CAP << 4); ++spins) __builtin_amdgcn_s_sleep(16);
    CFENCE();
    float fw[8];
    {
      const int h = l >> 5;
      float mw[8], lw[8];
#pragma unroll
      for (int ww = 0; ww < 8; ++ww) { mw[ww] = *(LAS float*)(L + L_ML + ww * 16 + h * 4); lw[ww] = *(LAS float*)(L + L_ML + ww * 16 + 8 + h * 4); }
      float M = mw[0];
#pragma unroll
      for (int ww = 1; ww < 8; ++ww) M = fmaxf(M, mw[ww]);
      float Ls = 0.f;
#pragma unroll
      for (int ww = 0; ww < 8; ++ww) { fw[ww] = __builtin_amdgcn_exp2f(mw[ww] - M); Ls = fmaf(fw[ww], lw[ww], Ls); }
      const float inv = 1.0f / Ls;
#pragma unroll
      for (int ww = 0; ww < 8; ++ww) { fw[ww] *= inv; asm volatile("" : "+v"(fw[ww])); }
    }
    int ooff = WS_OBOX + (kvh * 16 + b) * 1024 + l16;
    asm volatile("" : "+v"(ooff));
    __builtin_amdgcn_s_setprio(3);
    BARRIER();
    f32x4 ow[8];
#pragma unroll
    for (int ww = 0; ww < 8; ++ww) ow[ww] = *(LAS f32x4*)(L + L_RED + ww * 1024 + l16);
    const f32x4 O = ((fw[0] * ow[0] + fw[1] * ow[1]) + (fw[2] * ow[2] + fw[3] * ow[3])) + ((fw[4] * ow[4] + fw[5] * ow[5]) + (fw[6] * ow[6] + fw[7] * ow[7]));
    st_unit(rws, ooff, u32x4{pk_bf16(O.x, O.y), ta, pk_bf16(O.z, O.w), tb});
    if (oproj) BARRIER();
  } else {
    const __amdgpu_buffer_rsrc_t rk = make_rsrc(kc, B * HKV * SK * DH * 2);
    const __amdgpu_buffer_rsrc_t rv = make_rsrc(vc, B * HKV * SK * DH * 2);
    unsigned tp0 = t0 + (unsigned)(P_PACE0 + ((sw * P_PACE) >> 5));
    u32x4 R[64];
    const __amdgpu_buffer_rsrc_t rwo = make_rsrc(wo, NQ * D * 2);
    u32x4 wor[4];
    constexpr auto item = [](int j) constexpr { return j == 0 ? 0 : (j == 1 ? 32 : (j <= 32 ? j - 1 : j)); };
    constexpr auto pos = [](int i) constexpr { return i == 0 ? 0 : (i == 32 ? 1 : (i < 32 ? i + 1 : i)); };
#define ISSUE(j) do { constexpr int it_ = item(j);                                                                               \
      if constexpr ((j) >= W0) { wait_vm<WIN - 1>(); pace_until(tp0 + (unsigned)due(j)); SB(); }                                 \
      R[it_] = __builtin_amdgcn_raw_buffer_load_b128(it_ < 32 ? rk : rv, offa, (it_ * 8192 + rot8k) & 0x3ffff, AUX_NT); SB(); } while (0)
    auto phase_a = [&](auto self, auto jc) {
      constexpr int j = decltype(jc)::value;
      if constexpr (j < KB) {
        ISSUE(j);
        if constexpr (j == 1) {
#pragma unroll
          for (int u = 0; u < 4; ++u) { wor[u] = __builtin_amdgcn_raw_buffer_load_b128(rwo, offb, u * 8 * D * 2, AUX_NT); SB(); }
        }
        if constexpr (j == W0 - 1) { BARRIER(); EPOCH_GET(); }
        if constexpr (j == W0) {
#pragma unroll
          for (int u = 0; u < 4; ++u) *(LAS u32x4*)(L + L_WO + (sw * 32 + u * 8 + (l >> 3)) * ROWP + (l & 7) * 16) = wor[u];
          SB();
        }
        self(self, std::integral_constant<int, j + 1>{});
      }
    };
    phase_a(phase_a, std::integral_constant<int, 0>{});
    asm volatile("" ::"v"(pf));
    for (unsigned spins = 0; *(volatile LAS unsigned*)(L + L_FLAG) == 0u && spins < (SPIN_CAP << 4); ++spins) __builtin_amdgcn_s_sleep(1);
    CFENCE();
    {
      const int dt = (int)((unsigned)__builtin_amdgcn_readfirstlane(*(LAS unsigned*)(L + L_TREF)) - t0);
      tp0 = t0 + (unsigned)(min(max(dt, -400), 400) + P_PACE1 + ((sw * P_PACE) >> 5));
    }
    const u32x4 q0 = *(LAS u32x4*)(L + L_Q + c * 16), q1 = *(LAS u32x4*)(L + L_Q + 256 + c * 16);
    const bool up8 = (c & 8) != 0, up4 = (c & 4) != 0, up2 = (c & 2) != 0, up1 = (c & 1) != 0;
    float u0[4], u1[4], sc[2][2];
    const int hh = c >> 3, jj = c & 7;
    const unsigned pick = (unsigned)((2 * hh + 1) << 8 | (2 * hh));
    const unsigned act = (jj & 1) ? (pick << 16 | 0x0C0Cu) : (0x0C0C0000u | pick);
    u32x4 sel;
    sel.x = (jj >> 1) == 0 ? act : 0x0C0C0C0Cu; sel.y = (jj >> 1) == 1 ? act : 0x0C0C0C0Cu;
    sel.z = (jj >> 1) == 2 ? act : 0x0C0C0C0Cu; sel.w = (jj >> 1) == 3 ? act : 0x0C0C0C0Cu;
    unsigned P0 = 0, P1 = 0;
    f32x4 acc = {0.f, 0.f, 0.f, 0.f};
    u32x4 afr = {0u, 0u, 0u, 0u};
    constexpr float C2 = 0.088388347648318440f * 1.4426950408889634f;
#define COMB(UP, CTRL, LO, HI) ((UP ? HI : LO) + dpp<CTRL>(UP ? LO : HI))
#define AFRAG(I) do { const unsigned t_ = dppu<BCAST + bitrev4((I) & 15)>((I) < 16 ? P0 : P1);                                   \
      afr.x = __builtin_amdgcn_perm(t_, t_, sel.x); afr.y = __builtin_amdgcn_perm(t_, t_, sel.y);                                \
      afr.z = __builtin_amdgcn_perm(t_, t_, sel.z); afr.w = __builtin_amdgcn_perm(t_, t_, sel.w); } while (0)
    auto process = [&](auto ic) {
      constexpr int i = decltype(ic)::value;
      if constexpr (i < 32) {
        const u32x4 kk = R[i];
        float a = dot2(kk.x, q0.x, 0.f), bb = dot2(kk.x, q1.x, 0.f);
        a = dot2(kk.y, q0.y, a); bb = dot2(kk.y, q1.y, bb);
        a = dot2(kk.z, q0.z, a); bb = dot2(kk.z, q1.z, bb);
        a = dot2(kk.w, q0.w, a); bb = dot2(kk.w, q1.w, bb);
        if constexpr (!(i & 1)) { u0[0] = a; u1[0] = bb; }
        else {
          a = COMB(up8, XOR8, u0[0], a); bb = COMB(up8, XOR8, u1[0], bb);
          if constexpr (!(i & 2)) { u0[1] = a; u1[1] = bb; }
          else {
            a = COMB(up4, XOR7, u0[1], a); bb = COMB(up4, XOR7, u1[1], bb);
            if constexpr (!(i & 4)) { u0[2] = a; u1[2] = bb; }
            else {
              a = COMB(up2, XOR2, u0[2], a); bb = COMB(up2, XOR2, u1[2], bb);
              if constexpr (!(i & 8)) { u0[3] = a; u1[3] = bb; }
              else {
                a = COMB(up1, XOR1, u0[3], a); bb = COMB(up1, XOR1, u1[3], bb);
                sc[i >> 4][0] = a; sc[i >> 4][1] = bb;
              }
            }
          }
        }
        if constexpr (i == 31) {
          float m[2], lsum[2], p[2][2];
#pragma unroll
          for (int h = 0; h < 2; ++h) {
            const float s0 = sc[0][h] * C2, s1 = sc[1][h] * C2;
            m[h] = wave_max(fmaxf(s0, s1));
            p[0][h] = __builtin_amdgcn_exp2f(s0 - m[h]); p[1][h] = __builtin_amdgcn_exp2f(s1 - m[h]);
            lsum[h] = wave_sum(p[0][h] + p[1][h]);
          }
          P0 = pk_bf16(p[0][0], p[0][1]); P1 = pk_bf16(p[1][0], p[1][1]);
          if (l == 0) {
            *(LAS f32x4*)(L + L_ML + sw * 16) = f32x4{m[0], m[1], lsum[0], lsum[1]};
            CFENCE();
            *(volatile LAS unsigned*)(L + L_MLF + sw * 4) = 1u;
          }
          AFRAG(0);
        }
      } else {
        acc = mfma16(afr, R[i], acc);
        if constexpr (i < 63) AFRAG(i - 31);
      }
      SB();
    };
    auto phase_b = [&](auto self, auto jc, auto dc) {
      constexpr int j = decltype(jc)::value, done = decltype(dc)::value;
      if constexpr (done < 64) {
        constexpr int landed = j < 64 ? j - WIN : 64;
        auto run = [&](auto self2, auto ic) {
          constexpr int i = decltype(ic)::value;
          if constexpr (i < 64 && i < done + CHUNK && pos(i < 64 ? i : 0) < landed) { process(ic); return self2(self2, std::integral_constant<int, i + 1>{}); }
          else return ic;
        };
        auto nd = run(run, std::integral_constant<int, done>{});
        if constexpr (j < 64) ISSUE(j);
        self(self, std::integral_constant<int, (j < 64 ? j + 1 : 64)>{}, nd);
      }
    };
    phase_b(phase_b, std::integral_constant<int, KB>{}, std::integral_constant<int, 0>{});
#undef COMB
#undef AFRAG
#undef ISSUE
    *(LAS f32x4*)(L + L_RED + sw * 1024 + (g >> 1) * 512 + c * 32 + (g & 1) * 16) = acc;
    BARRIER();
    if (oproj) {
      const int off = (c & 7) * 1024 + sw * 128 + g * 32 + (c >> 3) * 16;
      __builtin_amdgcn_s_sleep(P_D1);
      SB();
      u32x4 a0 = ld_unit(rws, off, WS_OBOX + kvh * 16384), a1 = ld_unit(rws, off, WS_OBOX + kvh * 16384 + 8192);
      SB();
      u32x4 wfr[4];
#pragma unroll
      for (int t = 0; t < 4; ++t) wfr[t] = tr_frag(L + L_WO + (sw * 32 + 8 * g + (c >> 2)) * ROWP + t * 32 + (c & 3) * 8, 4 * ROWP);
      SB();
      {
        bool ok = (bad_unit(a0, ta, tb) | bad_unit(a1, ta, tb)) == 0u;
        for (unsigned spins = 0; !__all(ok) && spins < SPIN_CAP; ++spins) {
          CFENCE();
          if (!ok) {
            a0 = ld_unit(rws, off, WS_OBOX + kvh * 16384);
            a1 = ld_unit(rws, off, WS_OBOX + kvh * 16384 + 8192);
            ok = (bad_unit(a0, ta, tb) | bad_unit(a1, ta, tb)) == 0u;
          }
        }
      }
      const bool lo8 = c < 8;
      const unsigned gx = dppu<XOR8>(lo8 ? a1.x : a0.x), gz = dppu<XOR8>(lo8 ? a1.z : a0.z);
      const u32x4 of = {lo8 ? a0.x : gx, lo8 ? a0.z : gz, lo8 ? gx : a1.x, lo8 ? gz : a1.z};
#pragma unroll
      for (int t = 0; t < 4; ++t) {
        const f32x4 d = mfma16(wfr[t], of, f32x4{0.f, 0.f, 0.f, 0.f});
        *(LAS f32x4*)(L + L_RED2 + (sw * 4 + t) * 1024 + l16) = d;
      }
      BARRIER();
      if (sw < 4) {
        f32x4 s = *(LAS f32x4*)(L + L_RED2 + sw * 1024 + l16);
#pragma unroll
        for (int ww = 1; ww < 8; ++ww) s += *(LAS f32x4*)(L + L_RED2 + (ww * 4 + sw) * 1024 + l16);
        const bool lo = c < 8;
        const unsigned v0 = __float_as_uint(s.x), v1 = __float_as_uint(s.y), n0 = dppu<XOR8>(__float_as_uint(s.z)), n1 = dppu<XOR8>(__float_as_uint(s.w));
        const int base = WS_PBOX + (((c & 7) * 16 + kvh) * 288 + b * 32 + sw * 8 + g * 2) * 16;
        st_unit(rws, base + (lo ? 0 : 16), u32x4{lo ? v0 : n0, ta, lo ? v1 : n1, tb});
        st_unit(rws, base + 8 * 16 * 288 * 16 + (lo ? 16 : 0), u32x4{lo ? n0 : v0, ta, lo ? n1 : v1, tb});
      } else {
        const int poff = ((kvh * 16 + (l >> 3)) * 288 + b * 32 + (sw - 4) * 8 + (l & 7)) * 16;
        __builtin_amdgcn_s_sleep(P_D2);
        u32x4 p0 = ld_unit(rws, poff, WS_PBOX), p1 = ld_unit(rws, poff + 8 * 288 * 16, WS_PBOX);
        bool ok0 = bad_unit(p0, ta, tb) == 0u, ok1 = bad_unit(p1, ta, tb) == 0u;
        for (unsigned spins = 0; !__all(ok0 && ok1) && spins < SPIN_CAP; ++spins) {
          CFENCE();
          if (!ok0) p0 = ld_unit(rws, poff, WS_PBOX);
          if (!ok1) p1 = ld_unit(rws, poff + 8 * 288 * 16, WS_PBOX);
          ok0 = bad_unit(p0, ta, tb) == 0u; ok1 = bad_unit(p1, ta, tb) == 0u;
        }
        float v0 = __uint_as_float(p0.x) + __uint_as_float(p1.x), v1 = __uint_as_float(p0.z) + __uint_as_float(p1.z);
        v0 += dpp<XOR8>(v0); v1 += dpp<XOR8>(v1);
        v0 = xrow_sum(v0); v1 = xrow_sum(v1);
        if (l < 8) *(unsigned*)(out + kvh * D + b * 64 + (sw - 4) * 16 + l * 2) = pk_bf16(v0, v1);
        if (bid == 0 && tid == 5 * 64) __builtin_amdgcn_raw_buffer_store_b64(u32x2{ta, (unsigned)(e1 >> 32)}, rws, 0, WS_EPOCH, AUX_SC1);
      }
    }
  }
  asm volatile(".Lfused_code_end:");
}

extern "C" void kernel_launch(void* const* d_in, const int* in_sizes, int n_in, void* d_out, int out_size, void* d_ws,
                              size_t ws_size, hipStream_t stream) {
  fused_kernel<<<256, NWAVES * 64, 0, stream>>>((const unsigned short*)d_in[0], (const unsigned short*)d_in[1], (const unsigned short*)d_in[2],
                                                (const unsigned short*)d_in[3], (const unsigned short*)d_in[4], (unsigned short*)d_out,
                                                (unsigned char*)d_ws);
}
```
